```python
import torch

def _softmax(x, axis=-1):
    return torch.softmax(x, dim=axis)
def _gelu(x):
    return torch.nn.functional.gelu(x, approximate="tanh")
def _silu(x):
    return torch.nn.functional.silu(x)
def _sigmoid(x):
    return torch.sigmoid(x)
def _layernorm(x, axis=-1, eps=1e-5):
    m = x.mean(dim=axis, keepdim=True)
    v = x.var(dim=axis, keepdim=True, unbiased=False)
    return (x - m) / torch.sqrt(v + eps)
def _scatter_add(tgt, idx, src, axis=0):
    return tgt.clone().index_add_(axis, idx, src)
def _segment_sum(src, seg, n):
    out = torch.zeros((n,) + src.shape[1:], dtype=src.dtype)
    return out.index_add_(0, seg, src)
def _pad(x, pw):
    flat = tuple(v for pair in reversed(pw) for v in pair)
    return torch.nn.functional.pad(x, flat)

kernel_name = 'parallel_sum__mha_block__glu_gated_geglu__prompt'

def setup_inputs():

    _w = (lambda a: (lambda t: t.to(torch.bfloat16) if t.is_floating_point() else t)(torch.as_tensor(a)))
    def _A_setup():
        B, S, D, H = 1, 2048, 640, 32
        return {
            "x": _w((lambda *s: torch.randn(*s, dtype=torch.bfloat16) * 0.05)(B, S, D)),
            "wqkv": _w((lambda *s: torch.randn(*s, dtype=torch.bfloat16) * 0.05)(D, 3 * D)),
            "wo": _w((lambda *s: torch.randn(*s, dtype=torch.bfloat16) * 0.05)(D, D)),
        }
    _A_in = _A_setup()

    def _B_setup():
        B, S, Dm, Df = 1, 2048, 640, 1728
        return {
            "x": _w((lambda *s: torch.randn(*s, dtype=torch.bfloat16) * 0.05)(B, S, Dm)),
            "w_gate": _w((lambda *s: torch.randn(*s, dtype=torch.bfloat16) * 0.8)(Dm, Df) * (Dm ** -0.5)),
            "w_up": _w((lambda *s: torch.randn(*s, dtype=torch.bfloat16) * 0.8)(Dm, Df) * (Dm ** -0.5)),
            "w_down": _w((lambda *s: torch.randn(*s, dtype=torch.bfloat16) * 0.8)(Df, Dm) * (Df ** -0.5)),
        }
    _B_in = _B_setup()

    return {
        "A_x": _A_in["x"],
        "A_wqkv": _A_in["wqkv"],
        "A_wo": _A_in["wo"],
        "B_x": _B_in["x"],
        "B_w_gate": _B_in["w_gate"],
        "B_w_up": _B_in["w_up"],
        "B_w_down": _B_in["w_down"],
    }

def reference(A_x, A_wqkv, A_wo, B_x, B_w_gate, B_w_up, B_w_down):
    def _A_ref(x, wqkv, wo):
        B, S, D = x.shape
        H = 32
        Dh = D // H
        qkv = (lambda x, *d: x.permute(*d))((x @ wqkv).reshape(B, S, 3, H, Dh), 2, 0, 3, 1, 4)
        q, k, v = qkv[0], qkv[1], qkv[2]
        scores = torch.einsum("bhqd,bhkd->bhqk", q, k) * (Dh ** -0.5)
        p = _softmax(scores, axis=-1)
        o = torch.einsum("bhqk,bhkd->bhqd", p, v)
        return (lambda x, *d: x.permute(*d))(o, 0, 2, 1, 3).reshape(B, S, D) @ wo

    def _B_ref(x, w_gate, w_up, w_down):
        g = torch.einsum("bsd,df->bsf", x, w_gate)
        u = torch.einsum("bsd,df->bsf", x, w_up)
        h = _gelu(g) * u
        return torch.einsum("bsf,fd->bsd", h, w_down)

    x = A_x
    xn = _layernorm(x, axis=-1)
    return x + _A_ref(xn, A_wqkv, A_wo) + _B_ref(xn, B_w_gate, B_w_up, B_w_down)
```

```cpp
#include <hip/hip_runtime.h>
#include <hip/hip_bf16.h>
#include <stdint.h>
#include <type_traits>

typedef __attribute__((ext_vector_type(8))) short bf16x8;
typedef __attribute__((ext_vector_type(4))) short bf16x4;
typedef __attribute__((ext_vector_type(16))) float f32x16;
typedef __attribute__((ext_vector_type(4))) float f32x4;
typedef __attribute__((ext_vector_type(4))) unsigned u32x4;
typedef __attribute__((ext_vector_type(2))) unsigned u32x2;
typedef unsigned short u16;
typedef __attribute__((address_space(3))) char lds_char;

#define DI __device__ __forceinline__

constexpr int S_ = 2048, D_ = 640, NH = 32, DH = 20, F_ = 1728;
constexpr int KVT = 3584;
constexpr size_t WS_XN = 0;
constexpr size_t WS_Q = WS_XN + (size_t)S_ * D_ * 2;
constexpr size_t WS_KV = WS_Q + (size_t)NH * S_ * 24 * 2;
constexpr size_t WS_H = WS_KV + (size_t)NH * 64 * KVT;
constexpr size_t WS_O = WS_H + (size_t)S_ * F_ * 2;
constexpr size_t WS_WDT = WS_O + (size_t)S_ * D_ * 2;
constexpr size_t WS_WOT = WS_WDT + (size_t)D_ * F_ * 2;
constexpr size_t WS_DP = WS_WOT + (size_t)D_ * D_ * 2;
constexpr size_t WS_END = WS_DP + (size_t)S_ * D_ * 2;

typedef __attribute__((ext_vector_type(2))) __bf16 bf16x2_t;
typedef __attribute__((ext_vector_type(2))) float f32x2;
DI unsigned cvtpk(float lo, float hi) { return __builtin_bit_cast(unsigned, __builtin_convertvector((f32x2){lo, hi}, bf16x2_t)); }
DI float bf2f(u16 v) { return __uint_as_float(((unsigned)v) << 16); }
DI float bflo(unsigned v) { return __uint_as_float(v << 16); }
DI float bfhi(unsigned v) { return __uint_as_float(v & 0xffff0000u); }
#define MFMA32(a, b, c) __builtin_amdgcn_mfma_f32_32x32x16_bf16((a), (b), (c), 0, 0, 0)
#define MFMA16(a, b, c) __builtin_amdgcn_mfma_f32_16x16x32_bf16((a), (b), (c), 0, 0, 0)
#define MFMA8(a, b, c) __builtin_amdgcn_mfma_f32_32x32x8bf16_1k((a), (b), (c), 0, 0, 0)
typedef short v4i16_t __attribute__((ext_vector_type(4)));
DI bf16x4 tr_read(const lds_char* p) {
  return __builtin_bit_cast(bf16x4, __builtin_amdgcn_ds_read_tr16_b64_v4i16((__attribute__((address_space(3))) v4i16_t*)p));
}
DI void glds16(const void* g, unsigned lds_base) {
  unsigned sv;
  asm volatile("s_mov_b32 %0, m0\n\ts_mov_b32 m0, %2\n\ts_nop 0\n\tglobal_load_lds_dwordx4 %1, off\n\ts_mov_b32 m0, %0"
               : "=&s"(sv) : "v"(g), "s"(lds_base) : "memory");
}
DI void glds16s(unsigned voff, const void* sbase, unsigned lds_base) {
  unsigned sv;
  asm volatile("s_mov_b32 %0, m0\n\ts_mov_b32 m0, %3\n\ts_nop 0\n\tglobal_load_lds_dwordx4 %1, %2\n\ts_mov_b32 m0, %0"
               : "=&s"(sv) : "v"(voff), "s"(sbase), "s"(lds_base) : "memory");
}
#define WAIT_BAR(N) asm volatile("s_waitcnt vmcnt(" #N ") lgkmcnt(0)\n\ts_barrier" ::: "memory")

template <int I, int N, class F> DI void static_for(F&& f) {
  if constexpr (I < N) { f(std::integral_constant<int, I>{}); static_for<I + 1, N>(f); }
}
template <int N> DI void wait_bar() { asm volatile("s_waitcnt vmcnt(%0)\n\ts_barrier" ::"i"(N) : "memory"); }
constexpr int K1_NSLOT = 8, K1_NPIECE = 42;
constexpr bool k1_is_a(int pi) { return pi < 20 ? (pi & 1) == 0 : (pi - 20) % 11 == 9; }
constexpr int K1_SCR = K1_NSLOT * 16384;
constexpr int K1_LDS = K1_SCR + 2048 + 4 * 1024;
constexpr int k1_lo(int s) { return s < 10 ? 2 * s : 20 + 11 * (s / 10 - 1) + s % 10; }
constexpr int k1_hi(int s) { return s < 10 ? 2 * s + 1 : k1_lo(s) + (s % 10 == 9 ? 1 : 0); }
constexpr int k1_issued_before(int s) { return s == 0 ? 7 : (k1_lo(s - 1) + 7 < K1_NPIECE - 1 ? k1_lo(s - 1) + 7 : K1_NPIECE - 1); }
constexpr int k1_issued_after(int s) { return k1_lo(s) + 7 < K1_NPIECE - 1 ? k1_lo(s) + 7 : K1_NPIECE - 1; }
DI float dot2bf(unsigned a, unsigned b, float c) {
  return __builtin_amdgcn_fdot2_f32_bf16(__builtin_bit_cast(bf16x2_t, a), __builtin_bit_cast(bf16x2_t, b), c, false);
}

__global__ __launch_bounds__(256, 1) void k1_kernel(const u16* __restrict__ x, const u16* __restrict__ wqkv, const u16* __restrict__ wg,
                                                     const u16* __restrict__ wu, u16* __restrict__ Qb, char* __restrict__ KV, u16* __restrict__ H,
                                                     const u16* __restrict__ wd, const u16* __restrict__ wo, u16* __restrict__ wdT, u16* __restrict__ woT) {
  extern __shared__ __attribute__((aligned(16))) char smem[];
  lds_char* lds = (lds_char*)smem;
  const int tid = threadIdx.x, lane = tid & 63, wave = __builtin_amdgcn_readfirstlane(tid >> 6);
  const int r = lane & 31, hh = lane >> 5;
  const int bid = blockIdx.x;
  if (bid >= 224) {
    char* base = KV + (size_t)(bid - 224) * 64 * KVT;
    for (int i = tid; i < 64 * 32; i += 256) { const int t = i >> 5, k = i & 31; *(u32x2*)(base + (size_t)t * KVT + 1024 + 256 + k * 8) = (u32x2){0x3f80u, 0u}; }
    for (int i = tid; i < 64 * 4 * 12; i += 256) {
      const int t = i / 48, rem = i - t * 48, sh = rem / 12, d = 20 + rem - sh * 12;
      const unsigned v = d == 20 ? 0x3f803f80u : 0u;
      *(u32x4*)(base + (size_t)t * KVT + 1536 + sh * 512 + d * 16) = (u32x4){v, v, v, v};
    }
    u16* tl = (u16*)smem;
    for (int t = bid - 224; t < 370; t += 32) {
      const bool isd = t < 270;
      const int tt = isd ? t : t - 270, kb = tt / 10, nb = tt - kb * 10, ldk = isd ? F_ : D_;
      const u16* src = (isd ? wd : wo) + (size_t)(kb * 64) * D_ + nb * 64;
      u16* dst = (isd ? wdT : woT) + (size_t)(nb * 64) * ldk + kb * 64;
      __syncthreads();
#pragma unroll
      for (int i = 0; i < 2; ++i) {
        const int row = (tid >> 3) + 32 * i, c = (tid & 7) * 8;
        const u32x4 w = *(const u32x4*)(src + (size_t)row * D_ + c);
#pragma unroll
        for (int e = 0; e < 4; ++e) *(unsigned*)(tl + row * 66 + c + 2 * e) = w[e];
      }
      __syncthreads();
#pragma unroll
      for (int i = 0; i < 2; ++i) {
        const int n = (tid >> 3) + 32 * i, k = (tid & 7) * 8;
        u32x4 w;
#pragma unroll
        for (int e = 0; e < 4; ++e) w[e] = (unsigned)tl[(k + 2 * e) * 66 + n] | ((unsigned)tl[(k + 2 * e + 1) * 66 + n] << 16);
        *(u32x4*)(dst + (size_t)n * ldk + k) = w;
      }
    }
    return;
  }
  const int xcd = bid & 7, jj = bid >> 3;
  const int m0 = ((xcd & 3) * 4 + (jj & 3)) * 128, grp = (xcd >> 2) * 7 + (jj >> 2);
  const bool is_gu = grp >= 5;
  const int ldb = is_gu ? F_ : 1920;
  unsigned avoff, bvoff;
  {
    const int row = wave * 8 + (lane >> 3), cp = lane & 7;
    avoff = (unsigned)(row * D_ + (cp ^ ((row >> 1) & 7)) * 8) * 2u;
    bvoff = (unsigned)(row * ldb + (cp ^ (((row >> 1) & 1) << 2)) * 8) * 2u;
  }
  const char* abase = (const char*)(x + (size_t)m0 * D_);
  const char* bbase0 = nullptr;
  const char* bbase1 = nullptr;
  auto chunk_of = [&](int pass) { return is_gu ? grp * 3 + pass : (pass < 2 ? 2 * grp + pass : 10 + grp); };
  auto set_bp = [&](int pass) {
    const int chunk = chunk_of(pass);
    if (is_gu) { bbase0 = (const char*)(wg + (chunk - 15) * 64); bbase1 = (const char*)(wu + (chunk - 15) * 64); }
    else { bbase0 = (const char*)(wqkv + chunk * 128); bbase1 = bbase0 + 128; }
  };
  const unsigned ldsw = (unsigned)(uintptr_t)lds + wave * 1024;
  const long bhalf = 64L * ldb;
  auto issue_q = [&](auto ic, auto jc) {
    constexpr int pi = decltype(ic)::value, j = decltype(jc)::value;
    constexpr int slot = pi % K1_NSLOT;
    if constexpr (j == 0 && pi == 1) set_bp(0);
    if constexpr (j == 0 && pi == 20) set_bp(1);
    if constexpr (j == 0 && pi == 31) set_bp(2);
    if constexpr (k1_is_a(pi)) {
      glds16s(avoff, abase + j * 32 * D_ * 2, ldsw + slot * 16384 + j * 4096);
      if constexpr (j == 3 && pi < 18) abase += 128;
    } else {
      glds16s(bvoff, ((j & 1) ? bbase1 : bbase0) + ((j >> 1) ? bhalf : 0), ldsw + slot * 16384 + (j & 1) * 8192 + (j >> 1) * 4096);
      if constexpr (j == 3) { bbase0 += 2 * bhalf; bbase1 += 2 * bhalf; }
    }
  };
  auto issue = [&](auto ic) { static_for<0, 4>([&](auto jc) { issue_q(ic, jc); }); };
  static_for<0, 8>(issue);

  const int wr = wave >> 1, wc = wave & 1;
  const int arow = wr * 64 + r;
  const int aoffb = arow * 128 + ((hh ^ ((arow >> 1) & 1)) << 4), aswz = ((arow >> 1) & 6) << 4;
  int boff;
  {
    const int blk = (lane >> 4) & 1, q = (lane & 15) >> 2, p = lane & 3;
    boff = (8 * hh + q) * 128 + (((wc * 4 + 2 * blk + (p >> 1)) ^ ((q >> 1) << 2)) << 4) + 8 * (p & 1);
  }
  bf16x8 a[2][36], a9[2];
  f32x4 st1 = (f32x4){};
  f32x16 st2 = (f32x16){};
  bf16x8 sel;
  { const short v = ((lane & 15) == ((lane >> 4) & 1)) ? (short)0x3f80 : (short)0; sel = (bf16x8){v, v, v, v, v, v, v, v}; }
  float mr[2] = {0.f, 0.f}, rs[2] = {0.f, 0.f};
  float* scr_st = (float*)(smem + K1_SCR);
  float* scr_w = (float*)(smem + K1_SCR + 2048 + wave * 1024);
  struct B2 { bf16x8 f[2]; };
  auto bfrag2 = [&](const lds_char* p) {
    B2 o;
#pragma unroll
    for (int ni = 0; ni < 2; ++ni) {
      const lds_char* q = p + boff + ni * 8192;
      const bf16x4 lo = tr_read(q), hi = tr_read(q + 512);
      o.f[ni] = __builtin_shufflevector(lo, hi, 0, 1, 2, 3, 4, 5, 6, 7);
    }
    return o;
  };
  auto afrag = [&](const lds_char* p, int ks, int mi) { return *(const __attribute__((address_space(3))) bf16x8*)(p + aoffb + ((ks * 32) ^ aswz) + mi * 4096); };

  wait_bar<4 * (7 - 1)>();
#pragma unroll
  for (int ks = 0; ks < 4; ++ks) { a[0][ks] = afrag(lds, ks, 0); a[1][ks] = afrag(lds, ks, 1); }
  B2 bc = bfrag2(lds + 16384), bd = bfrag2(lds + 16384 + 2048);

  static_for<0, 3>([&](auto pc) {
    constexpr int PASS = decltype(pc)::value;
    const int chunk = chunk_of(PASS);
    const bool swap = PASS < 2 || is_gu;
    f32x16 acc[2][2] = {{(f32x16){}, (f32x16){}}, {(f32x16){}, (f32x16){}}};
    f32x4 cs[2] = {(f32x4){}, (f32x4){}};
    auto body = [&](auto swc) {
      constexpr bool SWAP = decltype(swc)::value;
      static_for<0, 10>([&](auto tc) {
        constexpr int t = decltype(tc)::value, s = PASS * 10 + t;
        constexpr int sn = s < 29 ? s + 1 : 29;
        wait_bar<4 * (k1_issued_before(s) - k1_hi(sn))>();
        const lds_char* ab = lds + (k1_lo(s) % K1_NSLOT) * 16384;
        const lds_char* bb = lds + (k1_hi(s) % K1_NSLOT) * 16384;
        const lds_char* abn = lds + (k1_lo(sn) % K1_NSLOT) * 16384;
        const lds_char* bbn = lds + (k1_hi(sn) % K1_NSLOT) * 16384;
        static_for<0, 4>([&](auto kc) {
          constexpr int ks = decltype(kc)::value;
          B2 bn;
          if constexpr (ks < 2) bn = bfrag2(bb + (ks + 2) * 2048);
          else if constexpr (s < 29) bn = bfrag2(bbn + (ks - 2) * 2048);
          if constexpr (s < 8) { a[0][4 * t + 4 + ks] = afrag(abn, ks, 0); a[1][4 * t + 4 + ks] = afrag(abn, ks, 1); }
          bf16x8 a0, a1;
          if constexpr (t < 9) { a0 = a[0][4 * t + ks]; a1 = a[1][4 * t + ks]; } else { a0 = a9[0]; a1 = a9[1]; }
          if constexpr (t == 8 && ks == 3) { a9[0] = afrag(abn, 0, 0); a9[1] = afrag(abn, 0, 1); }
          if constexpr (t == 9 && ks < 3) { a9[0] = afrag(ab, ks + 1, 0); a9[1] = afrag(ab, ks + 1, 1); }
          bf16x8 ast;
          if constexpr (PASS == 0) ast = afrag(ab + wc * 4096, ks, 0);
          acc[0][0] = SWAP ? MFMA32(bc.f[0], a0, acc[0][0]) : MFMA32(a0, bc.f[0], acc[0][0]);
          acc[0][1] = SWAP ? MFMA32(bc.f[1], a0, acc[0][1]) : MFMA32(a0, bc.f[1], acc[0][1]);
          {
            constexpr int np = k1_issued_after(s) - k1_issued_before(s), q0 = 4 * (k1_issued_before(s) + 1) + ks * np;
            static_for<q0, q0 + np>([&](auto qc) {
              constexpr int q = decltype(qc)::value;
              issue_q(std::integral_constant<int, q / 4>{}, std::integral_constant<int, q % 4>{});
            });
          }
          acc[1][0] = SWAP ? MFMA32(bc.f[0], a1, acc[1][0]) : MFMA32(a1, bc.f[0], acc[1][0]);
          acc[1][1] = SWAP ? MFMA32(bc.f[1], a1, acc[1][1]) : MFMA32(a1, bc.f[1], acc[1][1]);
          cs[0] = MFMA16(sel, bc.f[0], cs[0]);
          cs[1] = MFMA16(sel, bc.f[1], cs[1]);
          if constexpr (PASS == 0) { st1 = MFMA16(sel, ast, st1); st2 = MFMA32(ast, ast, st2); }
          __builtin_amdgcn_sched_barrier(0);
          bc = bd; bd = bn;
        });
      });
    };
    if constexpr (PASS < 2) body(std::true_type{});
    else { if (swap) body(std::true_type{}); else body(std::false_type{}); }

    if (lane < 16) { scr_w[lane] = cs[0][0]; scr_w[16 + lane] = cs[0][1]; scr_w[32 + lane] = cs[1][0]; scr_w[48 + lane] = cs[1][1]; }
    if constexpr (PASS == 0) {
      if (lane < 16) { scr_w[64 + lane] = st1[0]; scr_w[80 + lane] = st1[1]; }
      float dg = 0.f;
#pragma unroll
      for (int i = 0; i < 16; ++i) dg = (i == (r & 3) + 4 * (r >> 3)) ? st2[i] : dg;
      if (hh == ((r >> 2) & 1)) scr_w[96 + r] = dg;
      asm volatile("s_waitcnt lgkmcnt(0)" ::: "memory");
      const float t1 = scr_w[64 + r], t2 = scr_w[96 + r];
      const float mean = t1 * (1.f / D_);
      const float rstd = rsqrtf(fmaxf(t2 * (1.f / D_) - mean * mean, 0.f) + 1e-5f);
      if (hh == 0) { scr_st[arow + wc * 32] = mean * rstd; scr_st[128 + arow + wc * 32] = rstd; }
      asm volatile("s_waitcnt lgkmcnt(0)\n\ts_barrier" ::: "memory");
#pragma unroll
      for (int mi = 0; mi < 2; ++mi) { mr[mi] = scr_st[arow + mi * 32]; rs[mi] = scr_st[128 + arow + mi * 32]; }
    }
    if (swap) {
      if (is_gu) {
#pragma unroll
        for (int g = 0; g < 4; ++g) {
          const f32x4 c0 = *(const f32x4*)(scr_w + 8 * g + 4 * hh), c1 = *(const f32x4*)(scr_w + 32 + 8 * g + 4 * hh);
#pragma unroll
          for (int mi = 0; mi < 2; ++mi) {
            float hv[4];
#pragma unroll
            for (int i = 0; i < 4; ++i) {
              const float gg = acc[mi][0][4 * g + i] * rs[mi] - mr[mi] * c0[i], uu = acc[mi][1][4 * g + i] * rs[mi] - mr[mi] * c1[i];
              const float z = gg * (1.f + 0.044715f * gg * gg) * (-2.f * 0.7978845608028654f * 1.4426950408889634f);
              hv[i] = gg * __builtin_amdgcn_rcpf(1.f + __builtin_amdgcn_exp2f(z)) * uu;
            }
            const int f = (chunk - 15) * 64 + wc * 32 + 8 * g + 4 * hh;
            *(u32x2*)(H + (size_t)(m0 + arow + mi * 32) * F_ + f) = (u32x2){cvtpk(hv[0], hv[1]), cvtpk(hv[2], hv[3])};
          }
        }
      } else {
        const bool isq = chunk < 5;
        const float sc = isq ? 0.22360679774997896f * 1.4426950408889634f : 1.f;
        const int nb = (isq ? chunk : chunk - 5) * 128 + wc * 32 + 4 * hh;
#pragma unroll
        for (int ni = 0; ni < 2; ++ni)
#pragma unroll
          for (int g = 0; g < 4; ++g) {
            const f32x4 c0 = *(const f32x4*)(scr_w + 32 * ni + 8 * g + 4 * hh);
            const int n = nb + 64 * ni + 8 * g;
            const int head = n / DH, d = n - head * DH;
#pragma unroll
            for (int mi = 0; mi < 2; ++mi) {
              const int m = m0 + arow + mi * 32;
              const float r2 = rs[mi] * sc, m2 = mr[mi] * sc;
              const f32x16& ac = acc[mi][ni];
              const u32x2 w = {cvtpk(ac[4 * g] * r2 - m2 * c0[0], ac[4 * g + 1] * r2 - m2 * c0[1]), cvtpk(ac[4 * g + 2] * r2 - m2 * c0[2], ac[4 * g + 3] * r2 - m2 * c0[3])};
              if (isq) *(u32x2*)(Qb + ((size_t)(head * S_ + m)) * 24 + d) = w;
              else {
                char* base = KV + (size_t)(head * 64 + (m >> 5)) * KVT;
                const int off = d < 16 ? (d >> 3) * 512 + (m & 31) * 16 + (d & 7) * 2 : 1024 + (m & 31) * 8;
                *(u32x2*)(base + off) = w;
              }
            }
          }
      }
    } else {
#pragma unroll
      for (int mi = 0; mi < 2; ++mi) {
        f32x4 mrv[4], rsv[4];
#pragma unroll
        for (int g = 0; g < 4; ++g) {
          mrv[g] = *(const f32x4*)(scr_st + wr * 64 + mi * 32 + 8 * g + 4 * hh);
          rsv[g] = *(const f32x4*)(scr_st + 128 + wr * 64 + mi * 32 + 8 * g + 4 * hh);
        }
#pragma unroll
        for (int ni = 0; ni < 2; ++ni) {
          const float c0 = scr_w[32 * ni + r];
          const int n = (chunk - 10) * 128 + 64 * ni + 32 * wc + r;
          const int head = n / DH, d = n - head * DH;
          char* base = KV + (size_t)(head * 64 + ((m0 + wr * 64 + mi * 32) >> 5)) * KVT + 1536 + hh * 512 + d * 16;
          float v[16];
#pragma unroll
          for (int g = 0; g < 4; ++g)
#pragma unroll
            for (int i = 0; i < 4; ++i) v[4 * g + i] = acc[mi][ni][4 * g + i] * rsv[g][i] - mrv[g][i] * c0;
#pragma unroll
          for (int sx = 0; sx < 2; ++sx)
            *(u32x4*)(base + sx * 1024) = (u32x4){cvtpk(v[8 * sx], v[8 * sx + 1]), cvtpk(v[8 * sx + 2], v[8 * sx + 3]), cvtpk(v[8 * sx + 4], v[8 * sx + 5]), cvtpk(v[8 * sx + 6], v[8 * sx + 7])};
        }
      }
    }
  });
}

constexpr int ATT_TPS = 4;
constexpr int ATT_STAGE = ATT_TPS * KVT;
constexpr int ATT_NST = 4;
constexpr int ATT_LDS = ATT_NST * ATT_STAGE;
constexpr int ATT_NSTAGES = 64 / ATT_TPS;

DI float max16(const f32x16& s) {
  float a = fmaxf(fmaxf(s[0], s[1]), s[2]), b = fmaxf(fmaxf(s[3], s[4]), s[5]);
  a = fmaxf(fmaxf(a, s[6]), s[7]); b = fmaxf(fmaxf(b, s[8]), s[9]);
  a = fmaxf(fmaxf(a, s[10]), s[11]); b = fmaxf(fmaxf(b, s[12]), s[13]);
  a = fmaxf(fmaxf(a, s[14]), s[15]);
  return fmaxf(a, b);
}
DI float rowmax32(const f32x16& s) {
  const float mx = max16(s);
  auto rr = __builtin_amdgcn_permlane32_swap(__float_as_uint(mx), __float_as_uint(mx), false, false);
  return fmaxf(__uint_as_float(rr[0]), __uint_as_float(rr[1]));
}
DI void att_pv(f32x16& s, f32x16& o, const bf16x8& v0, const bf16x8& v1) {
#pragma unroll
  for (int i = 0; i < 16; ++i) s[i] = __builtin_amdgcn_exp2f(s[i]);
  const u32x4 p0 = {cvtpk(s[0], s[1]), cvtpk(s[2], s[3]), cvtpk(s[4], s[5]), cvtpk(s[6], s[7])};
  const u32x4 p1 = {cvtpk(s[8], s[9]), cvtpk(s[10], s[11]), cvtpk(s[12], s[13]), cvtpk(s[14], s[15])};
  o = MFMA32(v0, __builtin_bit_cast(bf16x8, p0), o);
  o = MFMA32(v1, __builtin_bit_cast(bf16x8, p1), o);
}
struct KFrag { bf16x8 k0; bf16x4 k1; };
struct VFrag { bf16x8 v0, v1; };
DI f32x16 att_qk(const KFrag& k, const bf16x8& q0, const bf16x4& q1) { return MFMA8(k.k1, q1, MFMA32(k.k0, q0, (f32x16){})); }

DI void att_stage(const char* kvsrc, lds_char* lds, int wave, int st, int slot) {
  const char* src = kvsrc + (size_t)st * ATT_STAGE;
  const unsigned d = (unsigned)(uintptr_t)lds + slot * ATT_STAGE + wave * 1024;
  glds16(src, d);
  if (wave < 6) glds16(src + 8192, d + 8192);
}

DI void att_loop_fast(const char* kvsrc, lds_char* lds, int wave, int lane, const bf16x8 (&qf0)[2], bf16x4 (&qf1)[2], float (&mref)[2], f32x16 (&o)[2]) {
  const int hh = lane >> 5, kh = wave >> 2;
  att_stage(kvsrc, lds, wave, 0, 0);
  att_stage(kvsrc, lds, wave, 1, 1);
  att_stage(kvsrc, lds, wave, 2, 2);
  const int k0off = kh * 2 * KVT + lane * 16, k1off = kh * 2 * KVT + 1024 + lane * 8, voff = kh * 2 * KVT + 1536 + lane * 16;
  auto rdk = [&](const lds_char* tb) { KFrag k; k.k0 = *(const __attribute__((address_space(3))) bf16x8*)(tb + k0off); k.k1 = *(const __attribute__((address_space(3))) bf16x4*)(tb + k1off); return k; };
  auto rdv = [&](const lds_char* tb) { VFrag v; v.v0 = *(const __attribute__((address_space(3))) bf16x8*)(tb + voff); v.v1 = *(const __attribute__((address_space(3))) bf16x8*)(tb + voff + 1024); return v; };
  if (wave < 6) WAIT_BAR(4); else WAIT_BAR(2);
  KFrag ka = rdk(lds);
  VFrag va = rdv(lds);
#pragma unroll
  for (int j = 0; j < 2; ++j) {
    const unsigned nb = cvtpk(rowmax32(att_qk(ka, qf0[j], qf1[j])), 0.f) & 0xffffu;
    mref[j] = __uint_as_float(nb << 16);
    if (hh) qf1[j][0] = (short)(nb ^ 0x8000u);
  }
  f32x16 sc = att_qk(ka, qf0[0], qf1[0]);
  int slot = 0;
  for (int st = 0; st < ATT_NSTAGES; ++st) {
    if (st + 2 < ATT_NSTAGES) { if (wave < 6) WAIT_BAR(2); else WAIT_BAR(1); } else WAIT_BAR(0);
    if (st + 3 < ATT_NSTAGES) att_stage(kvsrc, lds, wave, st + 3, (slot + 3) & 3);
    const lds_char* tb = lds + slot * ATT_STAGE;
    slot = (slot + 1) & 3;
    const lds_char* tn = lds + (st + 1 < ATT_NSTAGES ? slot : ((slot + 3) & 3)) * ATT_STAGE;
    const KFrag kb = rdk(tb + KVT);
    const VFrag vb = rdv(tb + KVT);
    f32x16 sn = att_qk(ka, qf0[1], qf1[1]);
    att_pv(sc, o[0], va.v0, va.v1);
    sc = att_qk(kb, qf0[0], qf1[0]);
    att_pv(sn, o[1], va.v0, va.v1);
    ka = rdk(tn);
    va = rdv(tn);
    sn = att_qk(kb, qf0[1], qf1[1]);
    att_pv(sc, o[0], vb.v0, vb.v1);
    sc = att_qk(ka, qf0[0], qf1[0]);
    att_pv(sn, o[1], vb.v0, vb.v1);
  }
}

DI void att_loop_safe(const char* kvsrc, lds_char* lds, int wave, int lane, const bf16x8 (&qf0)[2], const bf16x4 (&qf1)[2], float (&mref)[2], f32x16 (&o)[2]) {
  const int kh = wave >> 2;
  att_stage(kvsrc, lds, wave, 0, 0);
  att_stage(kvsrc, lds, wave, 1, 1);
  const int k0off = kh * 2 * KVT + lane * 16, k1off = kh * 2 * KVT + 1024 + lane * 8, voff = kh * 2 * KVT + 1536 + lane * 16;
  int slot = 0;
  for (int st = 0; st < ATT_NSTAGES; ++st) {
    if (st + 1 < ATT_NSTAGES) { if (wave < 6) WAIT_BAR(2); else WAIT_BAR(1); } else WAIT_BAR(0);
    if (st + 2 < ATT_NSTAGES) att_stage(kvsrc, lds, wave, st + 2, (slot + 2) & 3);
    const lds_char* tb = lds + slot * ATT_STAGE;
    slot = (slot + 1) & 3;
    for (int tt = 0; tt < 2; ++tt) {
      KFrag k; k.k0 = *(const __attribute__((address_space(3))) bf16x8*)(tb + k0off + tt * KVT); k.k1 = *(const __attribute__((address_space(3))) bf16x4*)(tb + k1off + tt * KVT);
      const bf16x8 v0 = *(const __attribute__((address_space(3))) bf16x8*)(tb + voff + tt * KVT);
      const bf16x8 v1 = *(const __attribute__((address_space(3))) bf16x8*)(tb + voff + tt * KVT + 1024);
#pragma unroll
      for (int j = 0; j < 2; ++j) {
        f32x16 sx = att_qk(k, qf0[j], qf1[j]);
        const float mnew = fmaxf(mref[j], rowmax32(sx));
        const float f = __builtin_amdgcn_exp2f(mref[j] - mnew);
#pragma unroll
        for (int i = 0; i < 16; ++i) { sx[i] -= mnew; o[j][i] *= f; }
        mref[j] = mnew;
        att_pv(sx, o[j], v0, v1);
      }
    }
  }
}

DI void attn_block(int bid, const u16* __restrict__ Qb, const char* __restrict__ KV, u16* __restrict__ O, char* smem) {
  lds_char* lds = (lds_char*)smem;
  const int tid = threadIdx.x, lane = tid & 63, wave = __builtin_amdgcn_readfirstlane(tid >> 6);
  const int r = lane & 31, hh = lane >> 5, kh = wave >> 2;
  const int head = (bid & 7) * 4 + ((bid >> 3) & 3), qb = bid >> 5;
  const int q0 = qb * 256 + (wave & 3) * 64 + r;
  const char* kvsrc = KV + (size_t)head * 64 * KVT + tid * 16;
  bf16x8 qf0[2];
  bf16x4 qf1[2], qf1i[2];
#pragma unroll
  for (int j = 0; j < 2; ++j) {
    const u16* qrow = Qb + ((size_t)head * S_ + q0 + 32 * j) * 24;
    qf0[j] = *(const bf16x8*)(qrow + hh * 8);
    const bf16x4 t = *(const bf16x4*)(qrow + 16);
    qf1i[j] = hh ? (bf16x4){0, 0, 0, 0} : t;
    qf1[j] = qf1i[j];
  }
  float mref[2] = {0.f, 0.f};
  f32x16 o[2] = {(f32x16){}, (f32x16){}};
  att_loop_fast(kvsrc, lds, wave, lane, qf0, qf1, mref, o);
  {
    bool bad = false;
#pragma unroll
    for (int j = 0; j < 2; ++j) {
#pragma unroll
      for (int i = 0; i < 12; ++i) bad |= !(fabsf(o[j][i]) < 0x1p120f);
      bad |= hh && !(o[j][8] > 0x1p-100f);
    }
    if (__syncthreads_or(bad)) {
#pragma unroll
      for (int j = 0; j < 2; ++j) { mref[j] = -0x1p126f; o[j] = (f32x16){}; }
      att_loop_safe(kvsrc, lds, wave, lane, qf0, qf1i, mref, o);
    }
  }
  __syncthreads();
  float* mg = (float*)smem + (wave & 3) * (26 * 64);
  if (kh) {
#pragma unroll
    for (int j = 0; j < 2; ++j) {
      mg[j * 13 * 64 + lane] = mref[j];
#pragma unroll
      for (int i = 0; i < 12; ++i) mg[(j * 13 + i + 1) * 64 + lane] = o[j][i];
    }
  }
  __syncthreads();
  if (kh) return;
#pragma unroll
  for (int j = 0; j < 2; ++j) {
    const float m2 = mg[j * 13 * 64 + lane];
    const float mm = fmaxf(mref[j], m2);
    const float f1 = __builtin_amdgcn_exp2f(mref[j] - mm), f2 = __builtin_amdgcn_exp2f(m2 - mm);
    float v[12];
#pragma unroll
    for (int i = 0; i < 12; ++i) v[i] = o[j][i] * f1 + mg[(j * 13 + i + 1) * 64 + lane] * f2;
    auto rr = __builtin_amdgcn_permlane32_swap(__float_as_uint(v[8]), __float_as_uint(v[8]), false, false);
    const float inv = 1.f / __uint_as_float(rr[1]);
    u16* orow = O + (size_t)(q0 + 32 * j) * D_ + head * DH + 4 * hh;
    *(u32x2*)(orow) = (u32x2){cvtpk(v[0] * inv, v[1] * inv), cvtpk(v[2] * inv, v[3] * inv)};
    *(u32x2*)(orow + 8) = (u32x2){cvtpk(v[4] * inv, v[5] * inv), cvtpk(v[6] * inv, v[7] * inv)};
    if (!hh) *(u32x2*)(orow + 16) = (u32x2){cvtpk(v[8] * inv, v[9] * inv), cvtpk(v[10] * inv, v[11] * inv)};
  }
}

template <int BM, int BN>
DI f32x16 nt_tile(const u16* __restrict__ A, int lda, const u16* __restrict__ B, int ldb, int nk, lds_char* lds, int wave, int lane) {
  constexpr int NW = (BM / 32) * (BN / 32), STG = (BM + BN) * 128, NI = (BM + BN) / 8 / NW;
  static_assert((BM + BN) / 8 % NW == 0 && BM / 8 % NW == 0, "tile");
  const int r = lane & 31, hh = lane >> 5, wm = wave % (BM / 32), wn = wave / (BM / 32);
  const int row8 = wave * 8 + (lane >> 3), cp = (lane & 7) ^ ((row8 >> 1) & 7);
  const unsigned avoff = (unsigned)(row8 * lda + cp * 8) * 2u, bvoff = (unsigned)(row8 * ldb + cp * 8) * 2u;
  const char* ab = (const char*)A;
  const char* bb = (const char*)B;
  const unsigned ldsw = (unsigned)(uintptr_t)lds + wave * 1024;
  auto issue = [&](int slot) {
#pragma unroll
    for (int i = 0; i < BM / 8 / NW; ++i) glds16s(avoff, ab + (size_t)i * NW * 8 * lda * 2, ldsw + slot * STG + i * NW * 1024);
#pragma unroll
    for (int i = 0; i < BN / 8 / NW; ++i) glds16s(bvoff, bb + (size_t)i * NW * 8 * ldb * 2, ldsw + slot * STG + BM * 128 + i * NW * 1024);
    ab += 128; bb += 128;
  };
  const int arow = wm * 32 + r, brow = wn * 32 + r;
  const int aoff = arow * 128 + ((hh ^ ((arow >> 1) & 1)) << 4), aswz = ((arow >> 1) & 6) << 4;
  const int boff = BM * 128 + brow * 128 + ((hh ^ ((brow >> 1) & 1)) << 4), bswz = ((brow >> 1) & 6) << 4;
  f32x16 acc = (f32x16){};
  issue(0);
  issue(1);
  int slot = 0;
  for (int kt = 0; kt < nk; ++kt) {
    if (kt + 1 < nk) asm volatile("s_waitcnt vmcnt(%0) lgkmcnt(0)\n\ts_barrier" ::"i"(NI) : "memory");
    else asm volatile("s_waitcnt vmcnt(0) lgkmcnt(0)\n\ts_barrier" ::: "memory");
    if (kt + 2 < nk) issue(slot == 0 ? 2 : slot - 1);
    const lds_char* sb = lds + slot * STG;
    slot = slot == 2 ? 0 : slot + 1;
#pragma unroll
    for (int ks = 0; ks < 4; ++ks) {
      const bf16x8 af = *(const __attribute__((address_space(3))) bf16x8*)(sb + aoff + ((ks * 32) ^ aswz));
      const bf16x8 bf = *(const __attribute__((address_space(3))) bf16x8*)(sb + boff + ((ks * 32) ^ bswz));
      acc = MFMA32(bf, af, acc);
    }
  }
  return acc;
}
DI void nt_store(const f32x16& acc, const u16* __restrict__ add, u16* __restrict__ out, int m, int n0, int hh) {
#pragma unroll
  for (int g = 0; g < 4; ++g) {
    const size_t o = (size_t)m * D_ + n0 + 8 * g + 4 * hh;
    const u32x2 xv = *(const u32x2*)(add + o);
    *(u32x2*)(out + o) = (u32x2){cvtpk(acc[4 * g] + bflo(xv[0]), acc[4 * g + 1] + bfhi(xv[0])), cvtpk(acc[4 * g + 2] + bflo(xv[1]), acc[4 * g + 3] + bfhi(xv[1]))};
  }
}

constexpr int K2_LDS = 3 * (128 + 64) * 128;
__global__ __launch_bounds__(512, 4) void k2_kernel(const u16* __restrict__ Qb, const char* __restrict__ KV, u16* __restrict__ O,
                                                     const u16* __restrict__ H, const u16* __restrict__ wdT, const u16* __restrict__ x, u16* __restrict__ dp) {
  extern __shared__ __attribute__((aligned(16))) char smem[];
  const int bid = blockIdx.x;
  if (bid < 256) { attn_block(bid, Qb, KV, O, smem); return; }
  const int b2 = bid - 256, xcd = b2 & 7, j = b2 >> 3;
  const int m0 = (xcd * 2 + (j & 1)) * 128, n0 = (j >> 1) * 64;
  const int lane = threadIdx.x & 63, wave = __builtin_amdgcn_readfirstlane(threadIdx.x >> 6);
  const f32x16 acc = nt_tile<128, 64>(H + (size_t)m0 * F_, F_, wdT + (size_t)n0 * F_, F_, F_ / 64, (lds_char*)smem, wave, lane);
  nt_store(acc, x, dp, m0 + (wave & 3) * 32 + (lane & 31), n0 + (wave >> 2) * 32, lane >> 5);
}

constexpr int K3_LDS = 3 * (64 + 64) * 128;
__global__ __launch_bounds__(256, 2) void k3_kernel(const u16* __restrict__ Ob, const u16* __restrict__ woT, const u16* __restrict__ dp, u16* __restrict__ out) {
  extern __shared__ __attribute__((aligned(16))) char smem[];
  const int bid = blockIdx.x, xcd = bid & 7, j = bid >> 3;
  const int m0 = (xcd * 4 + (j & 3)) * 64, n0 = (j >> 2) * 64;
  const int lane = threadIdx.x & 63, wave = __builtin_amdgcn_readfirstlane(threadIdx.x >> 6);
  const f32x16 acc = nt_tile<64, 64>(Ob + (size_t)m0 * D_, D_, woT + (size_t)n0 * D_, D_, D_ / 64, (lds_char*)smem, wave, lane);
  nt_store(acc, dp, out, m0 + (wave & 1) * 32 + (lane & 31), n0 + (wave >> 1) * 32, lane >> 5);
}

extern "C" void kernel_launch(void* const* d_in, const int* in_sizes, int n_in, void* d_out, int out_size, void* d_ws, size_t ws_size,
                              hipStream_t stream) {
  const u16* x = (const u16*)d_in[0];
  const u16* wqkv = (const u16*)d_in[1];
  const u16* wo = (const u16*)d_in[2];
  const u16* wg = (const u16*)d_in[4];
  const u16* wu = (const u16*)d_in[5];
  const u16* wd = (const u16*)d_in[6];
  char* ws = (char*)d_ws;
  u16* Qb = (u16*)(ws + WS_Q);
  char* KV = ws + WS_KV;
  u16* H = (u16*)(ws + WS_H);
  u16* Ob = (u16*)(ws + WS_O);
  u16* out = (u16*)d_out;
  u16* wdT = (u16*)(ws + WS_WDT);
  u16* woT = (u16*)(ws + WS_WOT);
  u16* dp = (u16*)(ws + WS_DP);
  (void)hipFuncSetAttribute((const void*)k1_kernel, hipFuncAttributeMaxDynamicSharedMemorySize, K1_LDS);
  (void)hipFuncSetAttribute((const void*)k2_kernel, hipFuncAttributeMaxDynamicSharedMemorySize, K2_LDS);
  (void)hipFuncSetAttribute((const void*)k3_kernel, hipFuncAttributeMaxDynamicSharedMemorySize, K3_LDS);
  k1_kernel<<<224 + 32, 256, K1_LDS, stream>>>(x, wqkv, wg, wu, Qb, KV, H, wd, wo, wdT, woT);
  k2_kernel<<<256 + 160, 512, K2_LDS, stream>>>(Qb, KV, Ob, H, wdT, x, dp);
  k3_kernel<<<320, 256, K3_LDS, stream>>>(Ob, woT, dp, out);
}
```

```cpp
#include <hip/hip_runtime.h>
#include <hip/hip_bf16.h>
#include <stdint.h>
#include <type_traits>

typedef __attribute__((ext_vector_type(8))) short bf16x8;
typedef __attribute__((ext_vector_type(4))) short bf16x4;
typedef __attribute__((ext_vector_type(16))) float f32x16;
typedef __attribute__((ext_vector_type(4))) float f32x4;
typedef __attribute__((ext_vector_type(4))) unsigned u32x4;
typedef __attribute__((ext_vector_type(2))) unsigned u32x2;
typedef unsigned short u16;
typedef __attribute__((address_space(3))) char lds_char;

#define DI __device__ __forceinline__

constexpr int S_ = 2048, D_ = 640, NH = 32, DH = 20, F_ = 1728;
constexpr int KVT = 3584;
constexpr size_t WS_XN = 0;
constexpr size_t WS_Q = WS_XN + (size_t)S_ * D_ * 2;
constexpr size_t WS_KV = WS_Q + (size_t)NH * S_ * 24 * 2;
constexpr size_t WS_H = WS_KV + (size_t)NH * 64 * KVT;
constexpr size_t WS_O = WS_H + (size_t)S_ * F_ * 2;
constexpr size_t WS_WDT = WS_O + (size_t)S_ * D_ * 2;
constexpr size_t WS_WOT = WS_WDT + (size_t)D_ * F_ * 2;
constexpr size_t WS_DP = WS_WOT + (size_t)D_ * D_ * 2;
constexpr size_t WS_END = WS_DP + (size_t)S_ * D_ * 2;

typedef __attribute__((ext_vector_type(2))) __bf16 bf16x2_t;
typedef __attribute__((ext_vector_type(2))) float f32x2;
DI unsigned cvtpk(float lo, float hi) { return __builtin_bit_cast(unsigned, __builtin_convertvector((f32x2){lo, hi}, bf16x2_t)); }
DI float bf2f(u16 v) { return __uint_as_float(((unsigned)v) << 16); }
DI float bflo(unsigned v) { return __uint_as_float(v << 16); }
DI float bfhi(unsigned v) { return __uint_as_float(v & 0xffff0000u); }
#define MFMA32(a, b, c) __builtin_amdgcn_mfma_f32_32x32x16_bf16((a), (b), (c), 0, 0, 0)
#define MFMA16(a, b, c) __builtin_amdgcn_mfma_f32_16x16x32_bf16((a), (b), (c), 0, 0, 0)
#define MFMA8(a, b, c) __builtin_amdgcn_mfma_f32_32x32x8bf16_1k((a), (b), (c), 0, 0, 0)
typedef short v4i16_t __attribute__((ext_vector_type(4)));
DI bf16x4 tr_read(const lds_char* p) {
  return __builtin_bit_cast(bf16x4, __builtin_amdgcn_ds_read_tr16_b64_v4i16((__attribute__((address_space(3))) v4i16_t*)p));
}
DI void glds16(const void* g, unsigned lds_base) {
  unsigned sv;
  asm volatile("s_mov_b32 %0, m0\n\ts_mov_b32 m0, %2\n\ts_nop 0\n\tglobal_load_lds_dwordx4 %1, off\n\ts_mov_b32 m0, %0"
               : "=&s"(sv) : "v"(g), "s"(lds_base) : "memory");
}
DI void glds16s(unsigned voff, const void* sbase, unsigned lds_base) {
  unsigned sv;
  asm volatile("s_mov_b32 %0, m0\n\ts_mov_b32 m0, %3\n\ts_nop 0\n\tglobal_load_lds_dwordx4 %1, %2\n\ts_mov_b32 m0, %0"
               : "=&s"(sv) : "v"(voff), "s"(sbase), "s"(lds_base) : "memory");
}
#define WAIT_BAR(N) asm volatile("s_waitcnt vmcnt(" #N ") lgkmcnt(0)\n\ts_barrier" ::: "memory")

template <int I, int N, class F> DI void static_for(F&& f) {
  if constexpr (I < N) { f(std::integral_constant<int, I>{}); static_for<I + 1, N>(f); }
}
template <int N> DI void wait_bar() { asm volatile("s_waitcnt vmcnt(%0)\n\ts_barrier" ::"i"(N) : "memory"); }
constexpr int K1_XOFF = 8 * 3 * 4096, K1_SCR = K1_XOFF + 6 * 8192;
constexpr int K1_LDS = K1_SCR + 1024 + 8 * 640;
constexpr int k1_wait(int s) {
  int pos = 0, need = 0;
  auto op = [&](bool isw, int idx, int n) { pos += n; if (isw ? (s < 10 && idx <= (s + 1 < 9 ? s + 1 : 9)) : (idx <= (s + 1 < 29 ? s + 1 : 29))) need = pos; };
  op(true, 0, 4); op(false, 0, 1); op(true, 1, 4); op(false, 1, 1); op(true, 2, 4); op(false, 2, 1); op(false, 3, 1); op(false, 4, 1);
  for (int t = 0; t < s; ++t) {
    if (t + 5 < 30) op(false, t + 5, 1);
    if (t + 3 < 10) op(true, t + 3, 4);
    if (t % 10 == 9) pos += 2;
  }
  return pos - need;
}

template <bool SWAP>
DI void k1_body(const u16* __restrict__ x, const u16* __restrict__ wqkv, const u16* __restrict__ wg, const u16* __restrict__ wu, u16* __restrict__ Qb,
                char* __restrict__ KV, u16* __restrict__ H, char* smem, int grp, int item, int wave, int lane) {
  lds_char* lds = (lds_char*)smem;
  const int r = lane & 31, hh = lane >> 5;
  const int T = grp * 8 + wave;
  const bool is_gu = T >= 60;
  const int ldb = is_gu ? F_ : 1920;
  const char* wp;
  {
    const int row = lane >> 2, c = lane & 3;
    const u16* w = is_gu ? ((c & 2) ? wu : wg) + (T - 60) * 16 + (c & 1) * 8 : wqkv + T * 32 + c * 8;
    wp = (const char*)(w + (size_t)row * ldb);
  }
  const long wstep = 32L * ldb;
  const unsigned wlds = (unsigned)(uintptr_t)lds + wave * 12288;
  auto issue_w = [&](int slot, int j) { glds16(wp, wlds + slot * 4096 + j * 1024); wp += wstep; };
  unsigned xvoff;
  { const int row = wave * 8 + (lane >> 3); xvoff = (unsigned)(row * D_ + ((lane & 7) ^ ((row >> 1) & 7)) * 8) * 2u; }
  auto rb_of = [&](int pass) { const int rb = item * 3 + pass; return rb < 31 ? rb : 31; };
  const unsigned xlds = (unsigned)(uintptr_t)lds + K1_XOFF + wave * 1024;
  auto issue_x = [&](auto sc) {
    constexpr int s = decltype(sc)::value;
    glds16s(xvoff, (const char*)x + (size_t)rb_of(s / 10) * 64 * D_ * 2 + (s % 10) * 128, xlds + (s % 6) * 8192);
  };
#pragma unroll
  for (int j = 0; j < 4; ++j) issue_w(0, j);
  issue_x(std::integral_constant<int, 0>{});
#pragma unroll
  for (int j = 0; j < 4; ++j) issue_w(1, j);
  issue_x(std::integral_constant<int, 1>{});
#pragma unroll
  for (int j = 0; j < 4; ++j) issue_w(2, j);
  issue_x(std::integral_constant<int, 2>{}); issue_x(std::integral_constant<int, 3>{}); issue_x(std::integral_constant<int, 4>{});

  const int aoffb = K1_XOFF + r * 128 + ((hh ^ ((r >> 1) & 1)) << 4), aswz = ((r >> 1) & 6) << 4;
  const int woff = wave * 12288 + (8 * hh + ((lane & 15) >> 2)) * 64 + ((lane >> 4) & 1) * 32 + (lane & 3) * 8;
  int soff;
  { const int row = 16 * (wave & 3) + (lane & 15), c = 4 * (wave >> 2) + (lane >> 4); soff = K1_XOFF + row * 128 + ((c ^ ((row >> 1) & 7)) << 4); }
  auto afrag = [&](int slot, int ks, int mi) { return *(const __attribute__((address_space(3))) bf16x8*)(lds + slot * 8192 + aoffb + ((ks * 32) ^ aswz) + mi * 4096); };
  auto wfrag = [&](int slot, int ks) {
    const bf16x4 lo = tr_read(lds + woff + slot * 4096 + ks * 1024), hi = tr_read(lds + woff + slot * 4096 + ks * 1024 + 256);
    return (bf16x8)__builtin_shufflevector(lo, hi, 0, 1, 2, 3, 4, 5, 6, 7);
  };
  bf16x8 sel;
  { const short v = ((lane & 15) == ((lane >> 4) & 1)) ? (short)0x3f80 : (short)0; sel = (bf16x8){v, v, v, v, v, v, v, v}; }

  bf16x8 w[40];
  wait_bar<k1_wait(-1)>();
#pragma unroll
  for (int ks = 0; ks < 4; ++ks) w[ks] = wfrag(0, ks);
  bf16x8 ac0 = afrag(0, 0, 0), ac1 = afrag(0, 0, 1);
  f32x4 cs = (f32x4){};

  static_for<0, 3>([&](auto pc) {
    constexpr int PASS = decltype(pc)::value;
    f32x16 acc[2] = {(f32x16){}, (f32x16){}};
    f32x4 st1 = (f32x4){}, st2 = (f32x4){};
    static_for<0, 10>([&](auto tc) {
      constexpr int t = decltype(tc)::value, s = PASS * 10 + t;
      wait_bar<k1_wait(s)>();
      if constexpr (s + 5 < 30) issue_x(std::integral_constant<int, s + 5>{});
      const bf16x8 sf = *(const __attribute__((address_space(3))) bf16x8*)(lds + (s % 6) * 8192 + soff);
      static_for<0, 4>([&](auto kc) {
        constexpr int ks = decltype(kc)::value;
        bf16x8 an0, an1;
        if constexpr (ks < 3) { an0 = afrag(s % 6, ks + 1, 0); an1 = afrag(s % 6, ks + 1, 1); }
        else if constexpr (t < 9) { an0 = afrag((s + 1) % 6, 0, 0); an1 = afrag((s + 1) % 6, 0, 1); }
        if constexpr (s < 9) w[4 * t + 4 + ks] = wfrag((t + 1) % 3, ks);
        acc[0] = SWAP ? MFMA32(w[4 * t + ks], ac0, acc[0]) : MFMA32(ac0, w[4 * t + ks], acc[0]);
        if constexpr (s + 3 < 10) issue_w(t % 3, ks);
        acc[1] = SWAP ? MFMA32(w[4 * t + ks], ac1, acc[1]) : MFMA32(ac1, w[4 * t + ks], acc[1]);
        if constexpr (PASS == 0) cs = MFMA16(sel, w[4 * t + ks], cs);
        if constexpr (ks == 2) { st1 = MFMA16(sel, sf, st1); st2 = MFMA16(sf, sf, st2); }
        __builtin_amdgcn_sched_barrier(0);
        ac0 = an0; ac1 = an1;
      });
    });
    int lz = lane;
    asm volatile("" : "+v"(lz));
    const int r = lz & 31, hh = lz >> 5;
    float* scr_p = (float*)(smem + K1_SCR);
    float* scr_c = (float*)(smem + K1_SCR + 1024 + wave * 640);
    float* scr_r = scr_c + 32;
    if constexpr (PASS == 0) { if (lz < 16) { scr_c[lz] = cs[0]; scr_c[16 + lz] = cs[1]; } }
    {
      float* pp = scr_p + ((wave >> 2) * 64 + 16 * (wave & 3)) * 2;
      if (lz < 16) pp[lz * 2] = st1[0] + st1[1];
      const int c = lz & 15;
      const float dg = (c & 3) == 0 ? st2[0] : ((c & 3) == 1 ? st2[1] : ((c & 3) == 2 ? st2[2] : st2[3]));
      if ((lz >> 4) == (c >> 2)) pp[c * 2 + 1] = dg;
    }
    asm volatile("s_waitcnt lgkmcnt(0)\n\ts_barrier" ::: "memory");
    auto row_stat = [&](int row, float& mr, float& rs) {
      const f32x2 p0 = *(const f32x2*)(scr_p + row * 2), p1 = *(const f32x2*)(scr_p + 128 + row * 2);
      const float mean = (p0[0] + p1[0]) * (1.f / D_);
      rs = rsqrtf(fmaxf((p0[1] + p1[1]) * (1.f / D_) - mean * mean, 0.f) + 1e-5f);
      mr = mean * rs;
    };
    const int m0 = rb_of(PASS) * 64;
    if constexpr (SWAP) {
#pragma unroll
      for (int mi = 0; mi < 2; ++mi) {
        const int m = m0 + mi * 32 + r;
        float mr, rs;
        row_stat(mi * 32 + r, mr, rs);
        if (is_gu) {
#pragma unroll
          for (int g = 0; g < 2; ++g) {
            const f32x4 c0 = *(const f32x4*)(scr_c + 8 * g + 4 * hh), c1 = *(const f32x4*)(scr_c + 16 + 8 * g + 4 * hh);
            float hv[4];
#pragma unroll
            for (int i = 0; i < 4; ++i) {
              const float gg = acc[mi][4 * g + i] * rs - mr * c0[i], uu = acc[mi][8 + 4 * g + i] * rs - mr * c1[i];
              const float z = gg * (gg * gg * (-2.f * 0.7978845608028654f * 1.4426950408889634f * 0.044715f) + (-2.f * 0.7978845608028654f * 1.4426950408889634f));
              hv[i] = gg * uu * __builtin_amdgcn_rcpf(1.f + __builtin_amdgcn_exp2f(z));
            }
            *(u32x2*)(smem + (mi * 32 + r) * 272 + wave * 32 + 16 * g + 8 * hh) = (u32x2){cvtpk(hv[0], hv[1]), cvtpk(hv[2], hv[3])};
          }
        } else {
          const bool isq = T < 20;
          const float sc = isq ? 0.22360679774997896f * 1.4426950408889634f : 1.f;
          const float r2 = rs * sc, m2 = mr * sc;
#pragma unroll
          for (int g = 0; g < 4; ++g) {
            const f32x4 c0 = *(const f32x4*)(scr_c + 8 * g + 4 * hh);
            const int n = (isq ? T : T - 20) * 32 + 8 * g + 4 * hh;
            const int head = n / DH, d = n - head * DH;
            const f32x16& v = acc[mi];
            const u32x2 o = {cvtpk(v[4 * g] * r2 - m2 * c0[0], v[4 * g + 1] * r2 - m2 * c0[1]), cvtpk(v[4 * g + 2] * r2 - m2 * c0[2], v[4 * g + 3] * r2 - m2 * c0[3])};
            const unsigned offq = (unsigned)((head * S_ + m) * 24 + d) * 2u;
            const unsigned offk = (unsigned)((head * 64 + (m >> 5)) * KVT + (d < 16 ? (d >> 3) * 512 + (m & 31) * 16 + (d & 7) * 2 : 1024 + (m & 31) * 8));
            *(u32x2*)((isq ? (char*)Qb : KV) + (isq ? offq : offk)) = o;
          }
        }
      }
    } else {
      { float mr, rs; row_stat(lz, mr, rs); scr_r[lz] = mr; scr_r[64 + lz] = rs; }
      const float c0 = scr_c[r];
      const int n = (T - 40) * 32 + r;
      const int head = n / DH, d = n - head * DH;
#pragma unroll
      for (int mi = 0; mi < 2; ++mi) {
        char* base = KV + (size_t)(head * 64 + ((m0 + mi * 32) >> 5)) * KVT + 1536 + hh * 512 + d * 16;
#pragma unroll
        for (int sx = 0; sx < 2; ++sx) {
          float v[8];
#pragma unroll
          for (int g2 = 0; g2 < 2; ++g2) {
            const int g = 2 * sx + g2;
            const f32x4 mrv = *(const f32x4*)(scr_r + mi * 32 + 8 * g + 4 * hh), rsv = *(const f32x4*)(scr_r + 64 + mi * 32 + 8 * g + 4 * hh);
#pragma unroll
            for (int i = 0; i < 4; ++i) v[4 * g2 + i] = acc[mi][4 * g + i] * rsv[i] - mrv[i] * c0;
          }
          *(u32x4*)(base + sx * 1024) = (u32x4){cvtpk(v[0], v[1]), cvtpk(v[2], v[3]), cvtpk(v[4], v[5]), cvtpk(v[6], v[7])};
        }
      }
    }
    if (grp >= 7) {
      asm volatile("s_waitcnt lgkmcnt(0)\n\ts_barrier" ::: "memory");
#pragma unroll
      for (int i = 0; i < 2; ++i) {
        const int row = wave * 8 + 4 * i + (lz >> 4), c = lz & 15, tc = grp * 8 + (c >> 1);
        if (tc >= 60) *(u32x4*)(H + (size_t)(m0 + row) * F_ + (tc - 60) * 16 + (c & 1) * 8) = *(const u32x4*)(smem + row * 272 + c * 16);
      }
    }
    if constexpr (PASS < 2) { ac0 = afrag((PASS * 10 + 10) % 6, 0, 0); ac1 = afrag((PASS * 10 + 10) % 6, 0, 1); }
  });
}

__global__ __launch_bounds__(512, 2) void k1_kernel(const u16* __restrict__ x, const u16* __restrict__ wqkv, const u16* __restrict__ wg,
                                                     const u16* __restrict__ wu, u16* __restrict__ Qb, char* __restrict__ KV, u16* __restrict__ H,
                                                     const u16* __restrict__ wd, const u16* __restrict__ wo, u16* __restrict__ wdT, u16* __restrict__ woT) {
  extern __shared__ __attribute__((aligned(16))) char smem[];
  const int tid = threadIdx.x, lane = tid & 63, wave = __builtin_amdgcn_readfirstlane(tid >> 6);
  const int bid = blockIdx.x;
  const int b2 = (bid & 7) * 29 + (bid >> 3);
  if (bid >= 232 || b2 >= 231) {
    const int sp = bid >= 232 ? bid - 231 : 0;
    for (int i = sp * 512 + tid; i < 2048 * 32; i += 25 * 512) { const int t = i >> 5, k = i & 31; *(u32x2*)(KV + (size_t)t * KVT + 1024 + 256 + k * 8) = (u32x2){0x3f80u, 0u}; }
    for (int i = sp * 512 + tid; i < 2048 * 48; i += 25 * 512) {
      const int t = i / 48, rem = i - t * 48, sh = rem / 12, d = 20 + rem - sh * 12;
      const unsigned v = d == 20 ? 0x3f803f80u : 0u;
      *(u32x4*)(KV + (size_t)t * KVT + 1536 + sh * 512 + d * 16) = (u32x4){v, v, v, v};
    }
    u16* tl = (u16*)smem;
    for (int t = sp; t < 370; t += 25) {
      const bool isd = t < 270;
      const int tt = isd ? t : t - 270, kb = tt / 10, nb = tt - kb * 10, ldk = isd ? F_ : D_;
      const u16* src = (isd ? wd : wo) + (size_t)(kb * 64) * D_ + nb * 64;
      u16* dst = (isd ? wdT : woT) + (size_t)(nb * 64) * ldk + kb * 64;
      __syncthreads();
      {
        const int row = tid >> 3, c = (tid & 7) * 8;
        const u32x4 w = *(const u32x4*)(src + (size_t)row * D_ + c);
#pragma unroll
        for (int e = 0; e < 4; ++e) *(unsigned*)(tl + row * 66 + c + 2 * e) = w[e];
      }
      __syncthreads();
      {
        const int n = tid >> 3, k = (tid & 7) * 8;
        u32x4 w;
#pragma unroll
        for (int e = 0; e < 4; ++e) w[e] = (unsigned)tl[(k + 2 * e) * 66 + n] | ((unsigned)tl[(k + 2 * e + 1) * 66 + n] << 16);
        *(u32x4*)(dst + (size_t)n * ldk + k) = w;
      }
    }
    return;
  }
  const int grp = b2 / 11, item = b2 - grp * 11;
  const int T = grp * 8 + wave;
  if (T >= 40 && T < 60) k1_body<false>(x, wqkv, wg, wu, Qb, KV, H, smem, grp, item, wave, lane);
  else k1_body<true>(x, wqkv, wg, wu, Qb, KV, H, smem, grp, item, wave, lane);
}

constexpr int ATT_TPS = 4;
constexpr int ATT_STAGE = ATT_TPS * KVT;
constexpr int ATT_NST = 4;
constexpr int ATT_LDS = ATT_NST * ATT_STAGE;
constexpr int ATT_NSTAGES = 64 / ATT_TPS;

DI float max16(const f32x16& s) {
  float a = fmaxf(fmaxf(s[0], s[1]), s[2]), b = fmaxf(fmaxf(s[3], s[4]), s[5]);
  a = fmaxf(fmaxf(a, s[6]), s[7]); b = fmaxf(fmaxf(b, s[8]), s[9]);
  a = fmaxf(fmaxf(a, s[10]), s[11]); b = fmaxf(fmaxf(b, s[12]), s[13]);
  a = fmaxf(fmaxf(a, s[14]), s[15]);
  return fmaxf(a, b);
}
DI float rowmax32(const f32x16& s) {
  const float mx = max16(s);
  auto rr = __builtin_amdgcn_permlane32_swap(__float_as_uint(mx), __float_as_uint(mx), false, false);
  return fmaxf(__uint_as_float(rr[0]), __uint_as_float(rr[1]));
}
DI void att_pv(f32x16& s, f32x16& o, const bf16x8& v0, const bf16x8& v1) {
#pragma unroll
  for (int i = 0; i < 16; ++i) s[i] = __builtin_amdgcn_exp2f(s[i]);
  const u32x4 p0 = {cvtpk(s[0], s[1]), cvtpk(s[2], s[3]), cvtpk(s[4], s[5]), cvtpk(s[6], s[7])};
  const u32x4 p1 = {cvtpk(s[8], s[9]), cvtpk(s[10], s[11]), cvtpk(s[12], s[13]), cvtpk(s[14], s[15])};
  o = MFMA32(v0, __builtin_bit_cast(bf16x8, p0), o);
  o = MFMA32(v1, __builtin_bit_cast(bf16x8, p1), o);
}
struct KFrag { bf16x8 k0; bf16x4 k1; };
struct VFrag { bf16x8 v0, v1; };
DI f32x16 att_qk(const KFrag& k, const bf16x8& q0, const bf16x4& q1) { return MFMA8(k.k1, q1, MFMA32(k.k0, q0, (f32x16){})); }

DI void att_stage(const char* kvsrc, lds_char* lds, int wave, int st, int slot) {
  const char* src = kvsrc + (size_t)st * ATT_STAGE;
  const unsigned d = (unsigned)(uintptr_t)lds + slot * ATT_STAGE + wave * 1024;
  glds16(src, d);
  if (wave < 6) glds16(src + 8192, d + 8192);
}

DI void att_loop_fast(const char* kvsrc, lds_char* lds, int wave, int lane, const bf16x8 (&qf0)[2], bf16x4 (&qf1)[2], float (&mref)[2], f32x16 (&o)[2]) {
  const int hh = lane >> 5, kh = wave >> 2;
  att_stage(kvsrc, lds, wave, 0, 0);
  att_stage(kvsrc, lds, wave, 1, 1);
  att_stage(kvsrc, lds, wave, 2, 2);
  const int k0off = kh * 2 * KVT + lane * 16, k1off = kh * 2 * KVT + 1024 + lane * 8, voff = kh * 2 * KVT + 1536 + lane * 16;
  auto rdk = [&](const lds_char* tb) { KFrag k; k.k0 = *(const __attribute__((address_space(3))) bf16x8*)(tb + k0off); k.k1 = *(const __attribute__((address_space(3))) bf16x4*)(tb + k1off); return k; };
  auto rdv = [&](const lds_char* tb) { VFrag v; v.v0 = *(const __attribute__((address_space(3))) bf16x8*)(tb + voff); v.v1 = *(const __attribute__((address_space(3))) bf16x8*)(tb + voff + 1024); return v; };
  if (wave < 6) WAIT_BAR(4); else WAIT_BAR(2);
  KFrag ka = rdk(lds);
  VFrag va = rdv(lds);
#pragma unroll
  for (int j = 0; j < 2; ++j) {
    const unsigned nb = cvtpk(rowmax32(att_qk(ka, qf0[j], qf1[j])), 0.f) & 0xffffu;
    mref[j] = __uint_as_float(nb << 16);
    if (hh) qf1[j][0] = (short)(nb ^ 0x8000u);
  }
  f32x16 sc = att_qk(ka, qf0[0], qf1[0]);
  int slot = 0;
  for (int st = 0; st < ATT_NSTAGES; ++st) {
    if (st + 2 < ATT_NSTAGES) { if (wave < 6) WAIT_BAR(2); else WAIT_BAR(1); } else WAIT_BAR(0);
    if (st + 3 < ATT_NSTAGES) att_stage(kvsrc, lds, wave, st + 3, (slot + 3) & 3);
    const lds_char* tb = lds + slot * ATT_STAGE;
    slot = (slot + 1) & 3;
    const lds_char* tn = lds + (st + 1 < ATT_NSTAGES ? slot : ((slot + 3) & 3)) * ATT_STAGE;
    const KFrag kb = rdk(tb + KVT);
    const VFrag vb = rdv(tb + KVT);
    f32x16 sn = att_qk(ka, qf0[1], qf1[1]);
    att_pv(sc, o[0], va.v0, va.v1);
    sc = att_qk(kb, qf0[0], qf1[0]);
    att_pv(sn, o[1], va.v0, va.v1);
    ka = rdk(tn);
    va = rdv(tn);
    sn = att_qk(kb, qf0[1], qf1[1]);
    att_pv(sc, o[0], vb.v0, vb.v1);
    sc = att_qk(ka, qf0[0], qf1[0]);
    att_pv(sn, o[1], vb.v0, vb.v1);
  }
}

DI void att_loop_safe(const char* kvsrc, lds_char* lds, int wave, int lane, const bf16x8 (&qf0)[2], const bf16x4 (&qf1)[2], float (&mref)[2], f32x16 (&o)[2]) {
  const int kh = wave >> 2;
  att_stage(kvsrc, lds, wave, 0, 0);
  att_stage(kvsrc, lds, wave, 1, 1);
  const int k0off = kh * 2 * KVT + lane * 16, k1off = kh * 2 * KVT + 1024 + lane * 8, voff = kh * 2 * KVT + 1536 + lane * 16;
  int slot = 0;
  for (int st = 0; st < ATT_NSTAGES; ++st) {
    if (st + 1 < ATT_NSTAGES) { if (wave < 6) WAIT_BAR(2); else WAIT_BAR(1); } else WAIT_BAR(0);
    if (st + 2 < ATT_NSTAGES) att_stage(kvsrc, lds, wave, st + 2, (slot + 2) & 3);
    const lds_char* tb = lds + slot * ATT_STAGE;
    slot = (slot + 1) & 3;
    for (int tt = 0; tt < 2; ++tt) {
      KFrag k; k.k0 = *(const __attribute__((address_space(3))) bf16x8*)(tb + k0off + tt * KVT); k.k1 = *(const __attribute__((address_space(3))) bf16x4*)(tb + k1off + tt * KVT);
      const bf16x8 v0 = *(const __attribute__((address_space(3))) bf16x8*)(tb + voff + tt * KVT);
      const bf16x8 v1 = *(const __attribute__((address_space(3))) bf16x8*)(tb + voff + tt * KVT + 1024);
#pragma unroll
      for (int j = 0; j < 2; ++j) {
        f32x16 sx = att_qk(k, qf0[j], qf1[j]);
        const float mnew = fmaxf(mref[j], rowmax32(sx));
        const float f = __builtin_amdgcn_exp2f(mref[j] - mnew);
#pragma unroll
        for (int i = 0; i < 16; ++i) { sx[i] -= mnew; o[j][i] *= f; }
        mref[j] = mnew;
        att_pv(sx, o[j], v0, v1);
      }
    }
  }
}

DI void attn_block(int bid, const u16* __restrict__ Qb, const char* __restrict__ KV, u16* __restrict__ O, char* smem) {
  lds_char* lds = (lds_char*)smem;
  const int tid = threadIdx.x, lane = tid & 63, wave = __builtin_amdgcn_readfirstlane(tid >> 6);
  const int r = lane & 31, hh = lane >> 5, kh = wave >> 2;
  const int head = (bid & 7) * 4 + ((bid >> 3) & 3), qb = bid >> 5;
  const int q0 = qb * 256 + (wave & 3) * 64 + r;
  const char* kvsrc = KV + (size_t)head * 64 * KVT + tid * 16;
  bf16x8 qf0[2];
  bf16x4 qf1[2], qf1i[2];
#pragma unroll
  for (int j = 0; j < 2; ++j) {
    const u16* qrow = Qb + ((size_t)head * S_ + q0 + 32 * j) * 24;
    qf0[j] = *(const bf16x8*)(qrow + hh * 8);
    const bf16x4 t = *(const bf16x4*)(qrow + 16);
    qf1i[j] = hh ? (bf16x4){0, 0, 0, 0} : t;
    qf1[j] = qf1i[j];
  }
  float mref[2] = {0.f, 0.f};
  f32x16 o[2] = {(f32x16){}, (f32x16){}};
  att_loop_fast(kvsrc, lds, wave, lane, qf0, qf1, mref, o);
  {
    bool bad = false;
#pragma unroll
    for (int j = 0; j < 2; ++j) {
#pragma unroll
      for (int i = 0; i < 12; ++i) bad |= !(fabsf(o[j][i]) < 0x1p120f);
      bad |= hh && !(o[j][8] > 0x1p-100f);
    }
    if (__syncthreads_or(bad)) {
#pragma unroll
      for (int j = 0; j < 2; ++j) { mref[j] = -0x1p126f; o[j] = (f32x16){}; }
      att_loop_safe(kvsrc, lds, wave, lane, qf0, qf1i, mref, o);
    }
  }
  __syncthreads();
  float* mg = (float*)smem + (wave & 3) * (26 * 64);
  if (kh) {
#pragma unroll
    for (int j = 0; j < 2; ++j) {
      mg[j * 13 * 64 + lane] = mref[j];
#pragma unroll
      for (int i = 0; i < 12; ++i) mg[(j * 13 + i + 1) * 64 + lane] = o[j][i];
    }
  }
  __syncthreads();
  if (kh) return;
#pragma unroll
  for (int j = 0; j < 2; ++j) {
    const float m2 = mg[j * 13 * 64 + lane];
    const float mm = fmaxf(mref[j], m2);
    const float f1 = __builtin_amdgcn_exp2f(mref[j] - mm), f2 = __builtin_amdgcn_exp2f(m2 - mm);
    float v[12];
#pragma unroll
    for (int i = 0; i < 12; ++i) v[i] = o[j][i] * f1 + mg[(j * 13 + i + 1) * 64 + lane] * f2;
    auto rr = __builtin_amdgcn_permlane32_swap(__float_as_uint(v[8]), __float_as_uint(v[8]), false, false);
    const float inv = 1.f / __uint_as_float(rr[1]);
    u16* orow = O + (size_t)(q0 + 32 * j) * D_ + head * DH + 4 * hh;
    *(u32x2*)(orow) = (u32x2){cvtpk(v[0] * inv, v[1] * inv), cvtpk(v[2] * inv, v[3] * inv)};
    *(u32x2*)(orow + 8) = (u32x2){cvtpk(v[4] * inv, v[5] * inv), cvtpk(v[6] * inv, v[7] * inv)};
    if (!hh) *(u32x2*)(orow + 16) = (u32x2){cvtpk(v[8] * inv, v[9] * inv), cvtpk(v[10] * inv, v[11] * inv)};
  }
}

template <int BM, int BN, int NST>
DI f32x16 nt_tile(const u16* __restrict__ A, int lda, const u16* __restrict__ B, int ldb, int nk, lds_char* lds, int wave, int lane) {
  constexpr int NW = (BM / 32) * (BN / 32), STG = (BM + BN) * 128, NI = (BM + BN) / 8 / NW;
  static_assert((BM + BN) / 8 % NW == 0 && BM / 8 % NW == 0 && NST >= 3 && NST <= 5, "tile");
  const int r = lane & 31, hh = lane >> 5, wm = wave % (BM / 32), wn = wave / (BM / 32);
  const int row8 = wave * 8 + (lane >> 3), cp = (lane & 7) ^ ((row8 >> 1) & 7);
  const unsigned avoff = (unsigned)(row8 * lda + cp * 8) * 2u, bvoff = (unsigned)(row8 * ldb + cp * 8) * 2u;
  const char* ab = (const char*)A;
  const char* bb = (const char*)B;
  const unsigned ldsw = (unsigned)(uintptr_t)lds + wave * 1024;
  auto issue = [&](int slot) {
#pragma unroll
    for (int i = 0; i < BM / 8 / NW; ++i) glds16s(avoff, ab + (size_t)i * NW * 8 * lda * 2, ldsw + slot * STG + i * NW * 1024);
#pragma unroll
    for (int i = 0; i < BN / 8 / NW; ++i) glds16s(bvoff, bb + (size_t)i * NW * 8 * ldb * 2, ldsw + slot * STG + BM * 128 + i * NW * 1024);
    ab += 128; bb += 128;
  };
  const int arow = wm * 32 + r, brow = wn * 32 + r;
  const int aoff = arow * 128 + ((hh ^ ((arow >> 1) & 1)) << 4), aswz = ((arow >> 1) & 6) << 4;
  const int boff = BM * 128 + brow * 128 + ((hh ^ ((brow >> 1) & 1)) << 4), bswz = ((brow >> 1) & 6) << 4;
  f32x16 acc = (f32x16){};
#pragma unroll
  for (int i = 0; i < NST - 1; ++i) issue(i);
  int slot = 0;
  for (int kt = 0; kt < nk; ++kt) {
    const int rem = nk - 1 - kt;
    if (rem >= NST - 2) asm volatile("s_waitcnt vmcnt(%0) lgkmcnt(0)\n\ts_barrier" ::"i"(NI * (NST - 2)) : "memory");
    else if (NST > 3 && rem == 2) asm volatile("s_waitcnt vmcnt(%0) lgkmcnt(0)\n\ts_barrier" ::"i"(NI * 2) : "memory");
    else if (rem == 1) asm volatile("s_waitcnt vmcnt(%0) lgkmcnt(0)\n\ts_barrier" ::"i"(NI) : "memory");
    else asm volatile("s_waitcnt vmcnt(0) lgkmcnt(0)\n\ts_barrier" ::: "memory");
    if (kt + NST - 1 < nk) issue(slot == 0 ? NST - 1 : slot - 1);
    const lds_char* sb = lds + slot * STG;
    slot = slot == NST - 1 ? 0 : slot + 1;
#pragma unroll
    for (int ks = 0; ks < 4; ++ks) {
      const bf16x8 af = *(const __attribute__((address_space(3))) bf16x8*)(sb + aoff + ((ks * 32) ^ aswz));
      const bf16x8 bf = *(const __attribute__((address_space(3))) bf16x8*)(sb + boff + ((ks * 32) ^ bswz));
      acc = MFMA32(bf, af, acc);
    }
  }
  return acc;
}
struct NtAdd { u32x2 v[4]; };
DI NtAdd nt_load(const u16* __restrict__ add, int m, int n0, int hh) {
  NtAdd r;
#pragma unroll
  for (int g = 0; g < 4; ++g) r.v[g] = *(const u32x2*)(add + (size_t)m * D_ + n0 + 8 * g + 4 * hh);
  return r;
}
DI void nt_store(const f32x16& acc, const NtAdd& ad, u16* __restrict__ out, int m, int n0, int hh) {
#pragma unroll
  for (int g = 0; g < 4; ++g) {
    const u32x2 xv = ad.v[g];
    *(u32x2*)(out + (size_t)m * D_ + n0 + 8 * g + 4 * hh) =
        (u32x2){cvtpk(acc[4 * g] + bflo(xv[0]), acc[4 * g + 1] + bfhi(xv[0])), cvtpk(acc[4 * g + 2] + bflo(xv[1]), acc[4 * g + 3] + bfhi(xv[1]))};
  }
}

constexpr int K2_LDS = 3 * (128 + 64) * 128;
__global__ __launch_bounds__(512, 4) void k2_kernel(const u16* __restrict__ Qb, const char* __restrict__ KV, u16* __restrict__ O,
                                                     const u16* __restrict__ H, const u16* __restrict__ wdT, const u16* __restrict__ x, u16* __restrict__ dp) {
  extern __shared__ __attribute__((aligned(16))) char smem[];
  const int bid = blockIdx.x;
  if (bid < 256) { attn_block(bid, Qb, KV, O, smem); return; }
  const int b2 = bid - 256, xcd = b2 & 7, j = b2 >> 3;
  const int m0 = (xcd * 2 + (j & 1)) * 128, n0 = (j >> 1) * 64;
  const int lane = threadIdx.x & 63, wave = __builtin_amdgcn_readfirstlane(threadIdx.x >> 6);
  const int m = m0 + (wave & 3) * 32 + (lane & 31), n = n0 + (wave >> 2) * 32;
  NtAdd ad = nt_load(x, m, n, lane >> 5);
  asm volatile("" : "+v"(ad.v[0]), "+v"(ad.v[1]), "+v"(ad.v[2]), "+v"(ad.v[3]));
  const f32x16 acc = nt_tile<128, 64, 3>(H + (size_t)m0 * F_, F_, wdT + (size_t)n0 * F_, F_, F_ / 64, (lds_char*)smem, wave, lane);
  nt_store(acc, ad, dp, m, n, lane >> 5);
}

constexpr int K3_LDS = 5 * (128 + 64) * 128;
__global__ __launch_bounds__(512, 2) void k3_kernel(const u16* __restrict__ Ob, const u16* __restrict__ woT, const u16* __restrict__ dp, u16* __restrict__ out) {
  extern __shared__ __attribute__((aligned(16))) char smem[];
  const int bid = blockIdx.x, xcd = bid & 7, j = bid >> 3;
  const int m0 = (xcd * 2 + (j & 1)) * 128, n0 = (j >> 1) * 64;
  const int lane = threadIdx.x & 63, wave = __builtin_amdgcn_readfirstlane(threadIdx.x >> 6);
  const int m = m0 + (wave & 3) * 32 + (lane & 31), n = n0 + (wave >> 2) * 32;
  const NtAdd ad = nt_load(dp, m, n, lane >> 5);
  const f32x16 acc = nt_tile<128, 64, 5>(Ob + (size_t)m0 * D_, D_, woT + (size_t)n0 * D_, D_, D_ / 64, (lds_char*)smem, wave, lane);
  nt_store(acc, ad, out, m, n, lane >> 5);
}

extern "C" void kernel_launch(void* const* d_in, const int* in_sizes, int n_in, void* d_out, int out_size, void* d_ws, size_t ws_size,
                              hipStream_t stream) {
  const u16* x = (const u16*)d_in[0];
  const u16* wqkv = (const u16*)d_in[1];
  const u16* wo = (const u16*)d_in[2];
  const u16* wg = (const u16*)d_in[4];
  const u16* wu = (const u16*)d_in[5];
  const u16* wd = (const u16*)d_in[6];
  char* ws = (char*)d_ws;
  u16* Qb = (u16*)(ws + WS_Q);
  char* KV = ws + WS_KV;
  u16* H = (u16*)(ws + WS_H);
  u16* Ob = (u16*)(ws + WS_O);
  u16* out = (u16*)d_out;
  u16* wdT = (u16*)(ws + WS_WDT);
  u16* woT = (u16*)(ws + WS_WOT);
  u16* dp = (u16*)(ws + WS_DP);
  (void)hipFuncSetAttribute((const void*)k1_kernel, hipFuncAttributeMaxDynamicSharedMemorySize, K1_LDS);
  (void)hipFuncSetAttribute((const void*)k2_kernel, hipFuncAttributeMaxDynamicSharedMemorySize, K2_LDS);
  (void)hipFuncSetAttribute((const void*)k3_kernel, hipFuncAttributeMaxDynamicSharedMemorySize, K3_LDS);
  k1_kernel<<<256, 512, K1_LDS, stream>>>(x, wqkv, wg, wu, Qb, KV, H, wd, wo, wdT, woT);
  k2_kernel<<<256 + 160, 512, K2_LDS, stream>>>(Qb, KV, Ob, H, wdT, x, dp);
  k3_kernel<<<160, 512, K3_LDS, stream>>>(Ob, woT, dp, out);
}
```

```cpp
#include <hip/hip_runtime.h>
#include <hip/hip_bf16.h>
#include <stdint.h>
#include <type_traits>

typedef __attribute__((ext_vector_type(8))) short bf16x8;
typedef __attribute__((ext_vector_type(4))) short bf16x4;
typedef __attribute__((ext_vector_type(16))) float f32x16;
typedef __attribute__((ext_vector_type(4))) float f32x4;
typedef __attribute__((ext_vector_type(4))) unsigned u32x4;
typedef __attribute__((ext_vector_type(2))) unsigned u32x2;
typedef unsigned short u16;
typedef __attribute__((address_space(3))) char lds_char;

#define DI __device__ __forceinline__

constexpr int S_ = 2048, D_ = 640, NH = 32, DH = 20, F_ = 1728;
constexpr int KVT = 3584;
constexpr size_t WS_Q = 0;
constexpr size_t WS_KV = WS_Q + (size_t)NH * S_ * 24 * 2;
constexpr size_t WS_H = WS_KV + (size_t)NH * 64 * KVT;
constexpr size_t WS_O = WS_H + (size_t)S_ * F_ * 2;
constexpr size_t WS_WDT = WS_O + (size_t)S_ * D_ * 2;
constexpr size_t WS_WOT = WS_WDT + (size_t)D_ * F_ * 2;
constexpr size_t WS_DP = WS_WOT + (size_t)D_ * D_ * 2;
constexpr size_t WS_END = WS_DP + (size_t)S_ * D_ * 2;

typedef __attribute__((ext_vector_type(2))) __bf16 bf16x2_t;
typedef __attribute__((ext_vector_type(2))) float f32x2;
DI unsigned cvtpk(float lo, float hi) { return __builtin_bit_cast(unsigned, __builtin_convertvector((f32x2){lo, hi}, bf16x2_t)); }
DI float bf2f(u16 v) { return __uint_as_float(((unsigned)v) << 16); }
DI float bflo(unsigned v) { return __uint_as_float(v << 16); }
DI float bfhi(unsigned v) { return __uint_as_float(v & 0xffff0000u); }
#define MFMA32(a, b, c) __builtin_amdgcn_mfma_f32_32x32x16_bf16((a), (b), (c), 0, 0, 0)
#define MFMA16(a, b, c) __builtin_amdgcn_mfma_f32_16x16x32_bf16((a), (b), (c), 0, 0, 0)
#define MFMA8(a, b, c) __builtin_amdgcn_mfma_f32_32x32x8bf16_1k((a), (b), (c), 0, 0, 0)
typedef short v4i16_t __attribute__((ext_vector_type(4)));
DI bf16x4 tr_read(const lds_char* p) {
  return __builtin_bit_cast(bf16x4, __builtin_amdgcn_ds_read_tr16_b64_v4i16((__attribute__((address_space(3))) v4i16_t*)p));
}
DI void glds16(const void* g, unsigned lds_base) {
  unsigned sv;
  asm volatile("s_mov_b32 %0, m0\n\ts_mov_b32 m0, %2\n\ts_nop 0\n\tglobal_load_lds_dwordx4 %1, off\n\ts_mov_b32 m0, %0"
               : "=&s"(sv) : "v"(g), "s"(lds_base) : "memory");
}
DI void glds16s(unsigned voff, const void* sbase, unsigned lds_base) {
  unsigned sv;
  asm volatile("s_mov_b32 %0, m0\n\ts_mov_b32 m0, %3\n\ts_nop 0\n\tglobal_load_lds_dwordx4 %1, %2\n\ts_mov_b32 m0, %0"
               : "=&s"(sv) : "v"(voff), "s"(sbase), "s"(lds_base) : "memory");
}
#define WAIT_BAR(N) asm volatile("s_waitcnt vmcnt(" #N ") lgkmcnt(0)\n\ts_barrier" ::: "memory")

template <int I, int N, class F> DI void static_for(F&& f) {
  if constexpr (I < N) { f(std::integral_constant<int, I>{}); static_for<I + 1, N>(f); }
}
template <int N> DI void wait_bar() { asm volatile("s_waitcnt vmcnt(%0)\n\ts_barrier" ::"i"(N) : "memory"); }
constexpr int K1_XOFF = 8 * 3 * 4096, K1_SCR = K1_XOFF + 5 * 8192;
constexpr int K1_LDS = K1_SCR + 1024 + 8 * 640;
constexpr int k1_wait(int s) {
  int pos = 0, need = 0;
  auto op = [&](bool isw, int idx, int n) { pos += n; if (isw ? (s < 10 && idx <= (s + 1 < 9 ? s + 1 : 9)) : (idx <= s + 1)) need = pos; };
  op(true, 0, 4); op(false, 0, 1); op(true, 1, 4); op(false, 1, 1); op(true, 2, 4); op(false, 2, 1); op(false, 3, 1);
  for (int t = 0; t < s; ++t) {
    op(false, t + 4, 1);
    if (t + 3 < 10) op(true, t + 3, 4);
    if (t % 10 == 9) pos += 2;
  }
  return pos - need;
}

template <bool SWAP>
DI void k1_body(const u16* __restrict__ x, const u16* __restrict__ wqkv, const u16* __restrict__ wg, const u16* __restrict__ wu, u16* __restrict__ Qb,
                char* __restrict__ KV, u16* __restrict__ H, char* smem, int grp, int item, int wave, int lane) {
  lds_char* lds = (lds_char*)smem;
  const int r = lane & 31, hh = lane >> 5;
  const int T = grp * 8 + wave;
  const bool is_gu = T >= 60;
  const char* wp[2];
  unsigned wadv[2];
#pragma unroll
  for (int e = 0; e < 2; ++e) {
    const int row = wave * 8 + 2 * e + hh, tile = grp * 8 + (((lane & 31) >> 2) ^ (row & 3)), c = lane & 3;
    const bool gu = tile >= 60;
    const u16* w = gu ? ((c & 2) ? wu : wg) + (tile - 60) * 16 + (c & 1) * 8 : wqkv + tile * 32 + c * 8;
    const int ld = gu ? F_ : 1920;
    wp[e] = (const char*)(w + (size_t)row * ld);
    wadv[e] = (unsigned)ld * 8u;
  }
  const unsigned wlds = (unsigned)(uintptr_t)lds + wave * 4096;
  auto issue_w = [&](int slot, int j) {
    glds16(wp[j & 1], wlds + slot * 32768 + j * 1024);
    wp[j & 1] += (j < 2) ? wadv[j & 1] : 15u * wadv[j & 1];
  };
  unsigned xvoff;
  { const int row = wave * 8 + (lane >> 3); xvoff = (unsigned)(row * D_ + ((lane & 7) ^ ((row >> 1) & 7)) * 8) * 2u; }
  auto rb_of = [&](int pass) { const int rb = item * 3 + pass; return rb < 31 ? rb : 31; };
  const unsigned xlds = (unsigned)(uintptr_t)lds + K1_XOFF + wave * 1024;
  auto issue_x = [&](int pass, auto tc) {
    constexpr int t = decltype(tc)::value;
    glds16s(xvoff, (const char*)x + (size_t)rb_of(pass + t / 10) * 64 * D_ * 2 + (t % 10) * 128, xlds + (t % 5) * 8192);
  };
#pragma unroll
  for (int j = 0; j < 4; ++j) issue_w(0, j);
  issue_x(0, std::integral_constant<int, 0>{});
#pragma unroll
  for (int j = 0; j < 4; ++j) issue_w(1, j);
  issue_x(0, std::integral_constant<int, 1>{});
#pragma unroll
  for (int j = 0; j < 4; ++j) issue_w(2, j);
  issue_x(0, std::integral_constant<int, 2>{}); issue_x(0, std::integral_constant<int, 3>{});

  const int aoffb = K1_XOFF + r * 128 + ((hh ^ ((r >> 1) & 1)) << 4), aswz = ((r >> 1) & 6) << 4;
  const int woff = (8 * hh + ((lane & 15) >> 2)) * 512 + ((wave ^ ((lane & 15) >> 2)) * 64) + ((lane >> 4) & 1) * 32 + (lane & 3) * 8;
  int soff;
  { const int row = 16 * (wave & 3) + (lane & 15), c = 4 * (wave >> 2) + (lane >> 4); soff = K1_XOFF + row * 128 + ((c ^ ((row >> 1) & 7)) << 4); }
  auto afrag = [&](int slot, int ks, int mi) { return *(const __attribute__((address_space(3))) bf16x8*)(lds + slot * 8192 + aoffb + ((ks * 32) ^ aswz) + mi * 4096); };
  auto wfrag = [&](int slot, int ks) {
    const bf16x4 lo = tr_read(lds + woff + slot * 32768 + ks * 8192), hi = tr_read(lds + woff + slot * 32768 + ks * 8192 + 2048);
    return (bf16x8)__builtin_shufflevector(lo, hi, 0, 1, 2, 3, 4, 5, 6, 7);
  };
  bf16x8 sel;
  { const short v = ((lane & 15) == ((lane >> 4) & 1)) ? (short)0x3f80 : (short)0; sel = (bf16x8){v, v, v, v, v, v, v, v}; }

  bf16x8 w[40];
  wait_bar<k1_wait(-1)>();
#pragma unroll
  for (int ks = 0; ks < 4; ++ks) w[ks] = wfrag(0, ks);
  bf16x8 ac0 = afrag(0, 0, 0), ac1 = afrag(0, 0, 1);
  f32x4 cs = (f32x4){};

  auto do_pass = [&](auto pc, int pass) {
    constexpr int PASS = decltype(pc)::value;
    f32x16 acc[2] = {(f32x16){}, (f32x16){}};
    f32x4 st1 = (f32x4){}, st2 = (f32x4){};
    static_for<0, 10>([&](auto tc) {
      constexpr int t = decltype(tc)::value, s = PASS * 10 + t;
      if constexpr (PASS == 0) asm volatile("s_waitcnt lgkmcnt(0)" ::: "memory");
      wait_bar<k1_wait(s)>();
      issue_x(pass, std::integral_constant<int, t + 4>{});
      const bf16x8 sf = *(const __attribute__((address_space(3))) bf16x8*)(lds + (t % 5) * 8192 + soff);
      static_for<0, 4>([&](auto kc) {
        constexpr int ks = decltype(kc)::value;
        bf16x8 an0, an1;
        if constexpr (ks < 3) { an0 = afrag(t % 5, ks + 1, 0); an1 = afrag(t % 5, ks + 1, 1); }
        else if constexpr (t < 9) { an0 = afrag((t + 1) % 5, 0, 0); an1 = afrag((t + 1) % 5, 0, 1); }
        if constexpr (s < 9) w[4 * t + 4 + ks] = wfrag((t + 1) % 3, ks);
        acc[0] = SWAP ? MFMA32(w[4 * t + ks], ac0, acc[0]) : MFMA32(ac0, w[4 * t + ks], acc[0]);
        if constexpr (s + 3 < 10) issue_w(t % 3, ks);
        acc[1] = SWAP ? MFMA32(w[4 * t + ks], ac1, acc[1]) : MFMA32(ac1, w[4 * t + ks], acc[1]);
        if constexpr (PASS == 0) cs = MFMA16(sel, w[4 * t + ks], cs);
        if constexpr (ks == 2) { st1 = MFMA16(sel, sf, st1); st2 = MFMA16(sf, sf, st2); }
        __builtin_amdgcn_sched_barrier(0);
        ac0 = an0; ac1 = an1;
      });
    });
    int lz = lane;
    asm volatile("" : "+v"(lz));
    const int r = lz & 31, hh = lz >> 5;
    float* scr_p = (float*)(smem + K1_SCR);
    float* scr_c = (float*)(smem + K1_SCR + 1024 + wave * 640);
    float* scr_r = scr_c + 32;
    if constexpr (PASS == 0) { if (lz < 16) { scr_c[lz] = cs[0]; scr_c[16 + lz] = cs[1]; } }
    {
      float* pp = scr_p + ((wave >> 2) * 64 + 16 * (wave & 3)) * 2;
      if (lz < 16) pp[lz * 2] = st1[0] + st1[1];
      const int c = lz & 15;
      const float dg = (c & 3) == 0 ? st2[0] : ((c & 3) == 1 ? st2[1] : ((c & 3) == 2 ? st2[2] : st2[3]));
      if ((lz >> 4) == (c >> 2)) pp[c * 2 + 1] = dg;
    }
    asm volatile("s_waitcnt lgkmcnt(0)\n\ts_barrier" ::: "memory");
    auto row_stat = [&](int row, float& mr, float& rs) {
      const f32x2 p0 = *(const f32x2*)(scr_p + row * 2), p1 = *(const f32x2*)(scr_p + 128 + row * 2);
      const float mean = (p0[0] + p1[0]) * (1.f / D_);
      rs = rsqrtf(fmaxf((p0[1] + p1[1]) * (1.f / D_) - mean * mean, 0.f) + 1e-5f);
      mr = mean * rs;
    };
    const int m0 = rb_of(pass) * 64;
    if constexpr (SWAP) {
#pragma unroll
      for (int mi = 0; mi < 2; ++mi) {
        const int m = m0 + mi * 32 + r;
        float mr, rs;
        row_stat(mi * 32 + r, mr, rs);
        if (is_gu) {
#pragma unroll
          for (int g = 0; g < 2; ++g) {
            const f32x4 c0 = *(const f32x4*)(scr_c + 8 * g + 4 * hh), c1 = *(const f32x4*)(scr_c + 16 + 8 * g + 4 * hh);
            float hv[4];
#pragma unroll
            for (int i = 0; i < 4; ++i) {
              const float gg = acc[mi][4 * g + i] * rs - mr * c0[i], uu = acc[mi][8 + 4 * g + i] * rs - mr * c1[i];
              const float z = gg * (gg * gg * (-2.f * 0.7978845608028654f * 1.4426950408889634f * 0.044715f) + (-2.f * 0.7978845608028654f * 1.4426950408889634f));
              hv[i] = gg * uu * __builtin_amdgcn_rcpf(1.f + __builtin_amdgcn_exp2f(z));
            }
            *(u32x2*)(smem + (mi * 32 + r) * 272 + wave * 32 + 16 * g + 8 * hh) = (u32x2){cvtpk(hv[0], hv[1]), cvtpk(hv[2], hv[3])};
          }
        } else {
          const bool isq = T < 20;
          const float sc = isq ? 0.22360679774997896f * 1.4426950408889634f : 1.f;
          const float r2 = rs * sc, m2 = mr * sc;
#pragma unroll
          for (int g = 0; g < 4; ++g) {
            const f32x4 c0 = *(const f32x4*)(scr_c + 8 * g + 4 * hh);
            const int n = (isq ? T : T - 20) * 32 + 8 * g + 4 * hh;
            const int head = n / DH, d = n - head * DH;
            const f32x16& v = acc[mi];
            const u32x2 o = {cvtpk(v[4 * g] * r2 - m2 * c0[0], v[4 * g + 1] * r2 - m2 * c0[1]), cvtpk(v[4 * g + 2] * r2 - m2 * c0[2], v[4 * g + 3] * r2 - m2 * c0[3])};
            const unsigned offq = (unsigned)((head * S_ + m) * 24 + d) * 2u;
            const unsigned offk = (unsigned)((head * 64 + (m >> 5)) * KVT + (d < 16 ? (d >> 3) * 512 + (m & 31) * 16 + (d & 7) * 2 : 1024 + (m & 31) * 8));
            *(u32x2*)((isq ? (char*)Qb : KV) + (isq ? offq : offk)) = o;
          }
        }
      }
    } else {
      { float mr, rs; row_stat(lz, mr, rs); scr_r[lz] = mr; scr_r[64 + lz] = rs; }
      const float c0 = scr_c[r];
      const int n = (T - 40) * 32 + r;
      const int head = n / DH, d = n - head * DH;
#pragma unroll
      for (int mi = 0; mi < 2; ++mi) {
        char* base = KV + (size_t)(head * 64 + ((m0 + mi * 32) >> 5)) * KVT + 1536 + hh * 512 + d * 16;
#pragma unroll
        for (int sx = 0; sx < 2; ++sx) {
          float v[8];
#pragma unroll
          for (int g2 = 0; g2 < 2; ++g2) {
            const int g = 2 * sx + g2;
            const f32x4 mrv = *(const f32x4*)(scr_r + mi * 32 + 8 * g + 4 * hh), rsv = *(const f32x4*)(scr_r + 64 + mi * 32 + 8 * g + 4 * hh);
#pragma unroll
            for (int i = 0; i < 4; ++i) v[4 * g2 + i] = acc[mi][4 * g + i] * rsv[i] - mrv[i] * c0;
          }
          *(u32x4*)(base + sx * 1024) = (u32x4){cvtpk(v[0], v[1]), cvtpk(v[2], v[3]), cvtpk(v[4], v[5]), cvtpk(v[6], v[7])};
        }
      }
    }
    if (grp >= 7) {
      asm volatile("s_waitcnt lgkmcnt(0)\n\ts_barrier" ::: "memory");
#pragma unroll
      for (int i = 0; i < 2; ++i) {
        const int row = wave * 8 + 4 * i + (lz >> 4), c = lz & 15, tc = grp * 8 + (c >> 1);
        if (tc >= 60) *(u32x4*)(H + (size_t)(m0 + row) * F_ + (tc - 60) * 16 + (c & 1) * 8) = *(const u32x4*)(smem + row * 272 + c * 16);
      }
    }
    ac0 = afrag(0, 0, 0); ac1 = afrag(0, 0, 1);
  };
  do_pass(std::integral_constant<int, 0>{}, 0);
#pragma unroll 1
  for (int pass = 1; pass < 3; ++pass) do_pass(std::integral_constant<int, 1>{}, pass);
  asm volatile("s_waitcnt vmcnt(0)" ::: "memory");
}

__global__ __launch_bounds__(512, 2) void k1_kernel(const u16* __restrict__ x, const u16* __restrict__ wqkv, const u16* __restrict__ wg,
                                                     const u16* __restrict__ wu, u16* __restrict__ Qb, char* __restrict__ KV, u16* __restrict__ H,
                                                     const u16* __restrict__ wd, const u16* __restrict__ wo, u16* __restrict__ wdT, u16* __restrict__ woT) {
  extern __shared__ __attribute__((aligned(16))) char smem[];
  const int tid = threadIdx.x, lane = tid & 63, wave = __builtin_amdgcn_readfirstlane(tid >> 6);
  const int bid = blockIdx.x;
  const int b2 = (bid & 7) * 29 + (bid >> 3);
  if (bid >= 232 || b2 >= 231) {
    const int sp = bid >= 232 ? bid - 231 : 0;
    for (int i = sp * 512 + tid; i < 2048 * 32; i += 25 * 512) { const int t = i >> 5, k = i & 31; *(u32x2*)(KV + (size_t)t * KVT + 1024 + 256 + k * 8) = (u32x2){0x3f80u, 0u}; }
    for (int i = sp * 512 + tid; i < 2048 * 48; i += 25 * 512) {
      const int t = i / 48, rem = i - t * 48, sh = rem / 12, d = 20 + rem - sh * 12;
      const unsigned v = d == 20 ? 0x3f803f80u : 0u;
      *(u32x4*)(KV + (size_t)t * KVT + 1536 + sh * 512 + d * 16) = (u32x4){v, v, v, v};
    }
    u16* tl = (u16*)smem;
    for (int t = sp; t < 370; t += 25) {
      const bool isd = t < 270;
      const int tt = isd ? t : t - 270, kb = tt / 10, nb = tt - kb * 10, ldk = isd ? F_ : D_;
      const u16* src = (isd ? wd : wo) + (size_t)(kb * 64) * D_ + nb * 64;
      u16* dst = (isd ? wdT : woT) + (size_t)(nb * 64) * ldk + kb * 64;
      __syncthreads();
      {
        const int row = tid >> 3, c = (tid & 7) * 8;
        const u32x4 w = *(const u32x4*)(src + (size_t)row * D_ + c);
#pragma unroll
        for (int e = 0; e < 4; ++e) *(unsigned*)(tl + row * 66 + c + 2 * e) = w[e];
      }
      __syncthreads();
      {
        const int n = tid >> 3, k = (tid & 7) * 8;
        u32x4 w;
#pragma unroll
        for (int e = 0; e < 4; ++e) w[e] = (unsigned)tl[(k + 2 * e) * 66 + n] | ((unsigned)tl[(k + 2 * e + 1) * 66 + n] << 16);
        *(u32x4*)(dst + (size_t)n * ldk + k) = w;
      }
    }
    return;
  }
  const int grp = b2 / 11, item = b2 - grp * 11;
  const int T = grp * 8 + wave;
  if (T >= 40 && T < 60) k1_body<false>(x, wqkv, wg, wu, Qb, KV, H, smem, grp, item, wave, lane);
  else k1_body<true>(x, wqkv, wg, wu, Qb, KV, H, smem, grp, item, wave, lane);
}

constexpr int ATT_TPS = 4;
constexpr int ATT_STAGE = ATT_TPS * KVT;
constexpr int ATT_NST = 4;
constexpr int ATT_LDS = ATT_NST * ATT_STAGE;
constexpr int ATT_NSTAGES = 64 / ATT_TPS;

DI float max16(const f32x16& s) {
  float a = fmaxf(fmaxf(s[0], s[1]), s[2]), b = fmaxf(fmaxf(s[3], s[4]), s[5]);
  a = fmaxf(fmaxf(a, s[6]), s[7]); b = fmaxf(fmaxf(b, s[8]), s[9]);
  a = fmaxf(fmaxf(a, s[10]), s[11]); b = fmaxf(fmaxf(b, s[12]), s[13]);
  a = fmaxf(fmaxf(a, s[14]), s[15]);
  return fmaxf(a, b);
}
DI float rowmax32(const f32x16& s) {
  const float mx = max16(s);
  auto rr = __builtin_amdgcn_permlane32_swap(__float_as_uint(mx), __float_as_uint(mx), false, false);
  return fmaxf(__uint_as_float(rr[0]), __uint_as_float(rr[1]));
}
DI void att_pv(f32x16& s, f32x16& o, const bf16x8& v0, const bf16x8& v1) {
#pragma unroll
  for (int i = 0; i < 16; ++i) s[i] = __builtin_amdgcn_exp2f(s[i]);
  const u32x4 p0 = {cvtpk(s[0], s[1]), cvtpk(s[2], s[3]), cvtpk(s[4], s[5]), cvtpk(s[6], s[7])};
  const u32x4 p1 = {cvtpk(s[8], s[9]), cvtpk(s[10], s[11]), cvtpk(s[12], s[13]), cvtpk(s[14], s[15])};
  o = MFMA32(v0, __builtin_bit_cast(bf16x8, p0), o);
  o = MFMA32(v1, __builtin_bit_cast(bf16x8, p1), o);
}
struct KFrag { bf16x8 k0; bf16x4 k1; };
struct VFrag { bf16x8 v0, v1; };
DI f32x16 att_qk(const KFrag& k, const bf16x8& q0, const bf16x4& q1) { return MFMA8(k.k1, q1, MFMA32(k.k0, q0, (f32x16){})); }

DI void att_stage(const char* kvsrc, lds_char* lds, int wave, int st, int slot) {
  const char* src = kvsrc + (size_t)st * ATT_STAGE;
  const unsigned d = (unsigned)(uintptr_t)lds + slot * ATT_STAGE + wave * 1024;
  glds16(src, d);
  if (wave < 6) glds16(src + 8192, d + 8192);
}

DI void att_loop_fast(const char* kvsrc, lds_char* lds, int wave, int lane, const bf16x8 (&qf0)[2], bf16x4 (&qf1)[2], float (&mref)[2], f32x16 (&o)[2]) {
  const int hh = lane >> 5, kh = wave >> 2;
  att_stage(kvsrc, lds, wave, 0, 0);
  att_stage(kvsrc, lds, wave, 1, 1);
  att_stage(kvsrc, lds, wave, 2, 2);
  const int k0off = kh * 2 * KVT + lane * 16, k1off = kh * 2 * KVT + 1024 + lane * 8, voff = kh * 2 * KVT + 1536 + lane * 16;
  auto rdk = [&](const lds_char* tb) { KFrag k; k.k0 = *(const __attribute__((address_space(3))) bf16x8*)(tb + k0off); k.k1 = *(const __attribute__((address_space(3))) bf16x4*)(tb + k1off); return k; };
  auto rdv = [&](const lds_char* tb) { VFrag v; v.v0 = *(const __attribute__((address_space(3))) bf16x8*)(tb + voff); v.v1 = *(const __attribute__((address_space(3))) bf16x8*)(tb + voff + 1024); return v; };
  if (wave < 6) WAIT_BAR(4); else WAIT_BAR(2);
  KFrag ka = rdk(lds);
  VFrag va = rdv(lds);
#pragma unroll
  for (int j = 0; j < 2; ++j) {
    const unsigned nb = cvtpk(rowmax32(att_qk(ka, qf0[j], qf1[j])), 0.f) & 0xffffu;
    mref[j] = __uint_as_float(nb << 16);
    if (hh) qf1[j][0] = (short)(nb ^ 0x8000u);
  }
  f32x16 sc = att_qk(ka, qf0[0], qf1[0]);
  int slot = 0;
  for (int st = 0; st < ATT_NSTAGES; ++st) {
    if (st + 2 < ATT_NSTAGES) { if (wave < 6) wait_bar<2>(); else wait_bar<1>(); } else wait_bar<0>();
    if (st + 3 < ATT_NSTAGES) att_stage(kvsrc, lds, wave, st + 3, (slot + 3) & 3);
    const lds_char* tb = lds + slot * ATT_STAGE;
    slot = (slot + 1) & 3;
    const lds_char* tn = lds + (st + 1 < ATT_NSTAGES ? slot : ((slot + 3) & 3)) * ATT_STAGE;
    const KFrag kb = rdk(tb + KVT);
    const VFrag vb = rdv(tb + KVT);
    f32x16 sn = att_qk(ka, qf0[1], qf1[1]);
    att_pv(sc, o[0], va.v0, va.v1);
    sc = att_qk(kb, qf0[0], qf1[0]);
    att_pv(sn, o[1], va.v0, va.v1);
    ka = rdk(tn);
    va = rdv(tn);
    sn = att_qk(kb, qf0[1], qf1[1]);
    att_pv(sc, o[0], vb.v0, vb.v1);
    sc = att_qk(ka, qf0[0], qf1[0]);
    att_pv(sn, o[1], vb.v0, vb.v1);
  }
}

DI void att_loop_safe(const char* kvsrc, lds_char* lds, int wave, int lane, const bf16x8 (&qf0)[2], const bf16x4 (&qf1)[2], float (&mref)[2], f32x16 (&o)[2]) {
  const int kh = wave >> 2;
  att_stage(kvsrc, lds, wave, 0, 0);
  att_stage(kvsrc, lds, wave, 1, 1);
  const int k0off = kh * 2 * KVT + lane * 16, k1off = kh * 2 * KVT + 1024 + lane * 8, voff = kh * 2 * KVT + 1536 + lane * 16;
  int slot = 0;
  for (int st = 0; st < ATT_NSTAGES; ++st) {
    if (st + 1 < ATT_NSTAGES) { if (wave < 6) WAIT_BAR(2); else WAIT_BAR(1); } else WAIT_BAR(0);
    if (st + 2 < ATT_NSTAGES) att_stage(kvsrc, lds, wave, st + 2, (slot + 2) & 3);
    const lds_char* tb = lds + slot * ATT_STAGE;
    slot = (slot + 1) & 3;
    for (int tt = 0; tt < 2; ++tt) {
      KFrag k; k.k0 = *(const __attribute__((address_space(3))) bf16x8*)(tb + k0off + tt * KVT); k.k1 = *(const __attribute__((address_space(3))) bf16x4*)(tb + k1off + tt * KVT);
      const bf16x8 v0 = *(const __attribute__((address_space(3))) bf16x8*)(tb + voff + tt * KVT);
      const bf16x8 v1 = *(const __attribute__((address_space(3))) bf16x8*)(tb + voff + tt * KVT + 1024);
#pragma unroll
      for (int j = 0; j < 2; ++j) {
        f32x16 sx = att_qk(k, qf0[j], qf1[j]);
        const float mnew = fmaxf(mref[j], rowmax32(sx));
        const float f = __builtin_amdgcn_exp2f(mref[j] - mnew);
#pragma unroll
        for (int i = 0; i < 16; ++i) { sx[i] -= mnew; o[j][i] *= f; }
        mref[j] = mnew;
        att_pv(sx, o[j], v0, v1);
      }
    }
  }
}

DI void attn_block(int bid, const u16* __restrict__ Qb, const char* __restrict__ KV, u16* __restrict__ O, char* smem) {
  lds_char* lds = (lds_char*)smem;
  const int tid = threadIdx.x, lane = tid & 63, wave = __builtin_amdgcn_readfirstlane(tid >> 6);
  const int r = lane & 31, hh = lane >> 5, kh = wave >> 2;
  const int head = (bid & 7) * 4 + ((bid >> 3) & 3), qb = bid >> 5;
  const int q0 = qb * 256 + (wave & 3) * 64 + r;
  const char* kvsrc = KV + (size_t)head * 64 * KVT + tid * 16;
  bf16x8 qf0[2];
  bf16x4 qf1[2], qf1i[2];
#pragma unroll
  for (int j = 0; j < 2; ++j) {
    const u16* qrow = Qb + ((size_t)head * S_ + q0 + 32 * j) * 24;
    qf0[j] = *(const bf16x8*)(qrow + hh * 8);
    const bf16x4 t = *(const bf16x4*)(qrow + 16);
    qf1i[j] = hh ? (bf16x4){0, 0, 0, 0} : t;
    qf1[j] = qf1i[j];
  }
  float mref[2] = {0.f, 0.f};
  f32x16 o[2] = {(f32x16){}, (f32x16){}};
  att_loop_fast(kvsrc, lds, wave, lane, qf0, qf1, mref, o);
  {
    bool bad = false;
#pragma unroll
    for (int j = 0; j < 2; ++j) {
#pragma unroll
      for (int i = 0; i < 12; ++i) bad |= !(fabsf(o[j][i]) < 0x1p120f);
      bad |= hh && !(o[j][8] > 0x1p-100f);
    }
    if (__syncthreads_or(bad)) {
#pragma unroll
      for (int j = 0; j < 2; ++j) { mref[j] = -0x1p126f; o[j] = (f32x16){}; }
      att_loop_safe(kvsrc, lds, wave, lane, qf0, qf1i, mref, o);
    }
  }
  __syncthreads();
  float* mg = (float*)smem + (wave & 3) * (26 * 64);
  if (kh) {
#pragma unroll
    for (int j = 0; j < 2; ++j) {
      mg[j * 13 * 64 + lane] = mref[j];
#pragma unroll
      for (int i = 0; i < 12; ++i) mg[(j * 13 + i + 1) * 64 + lane] = o[j][i];
    }
  }
  __syncthreads();
  if (kh) return;
#pragma unroll
  for (int j = 0; j < 2; ++j) {
    const float m2 = mg[j * 13 * 64 + lane];
    const float mm = fmaxf(mref[j], m2);
    const float f1 = __builtin_amdgcn_exp2f(mref[j] - mm), f2 = __builtin_amdgcn_exp2f(m2 - mm);
    float v[12];
#pragma unroll
    for (int i = 0; i < 12; ++i) v[i] = o[j][i] * f1 + mg[(j * 13 + i + 1) * 64 + lane] * f2;
    auto rr = __builtin_amdgcn_permlane32_swap(__float_as_uint(v[8]), __float_as_uint(v[8]), false, false);
    const float inv = 1.f / __uint_as_float(rr[1]);
    u16* orow = O + (size_t)(q0 + 32 * j) * D_ + head * DH + 4 * hh;
    *(u32x2*)(orow) = (u32x2){cvtpk(v[0] * inv, v[1] * inv), cvtpk(v[2] * inv, v[3] * inv)};
    *(u32x2*)(orow + 8) = (u32x2){cvtpk(v[4] * inv, v[5] * inv), cvtpk(v[6] * inv, v[7] * inv)};
    if (!hh) *(u32x2*)(orow + 16) = (u32x2){cvtpk(v[8] * inv, v[9] * inv), cvtpk(v[10] * inv, v[11] * inv)};
  }
}

template <int BM, int BN, int NST>
DI f32x16 nt_tile(const u16* __restrict__ A, int lda, const u16* __restrict__ B, int ldb, int nk, lds_char* lds, int wave, int lane) {
  constexpr int NW = (BM / 32) * (BN / 32), STG = (BM + BN) * 128, NI = (BM + BN) / 8 / NW;
  static_assert((BM + BN) / 8 % NW == 0 && BM / 8 % NW == 0 && NST >= 3 && NST <= 5, "tile");
  const int r = lane & 31, hh = lane >> 5, wm = wave % (BM / 32), wn = wave / (BM / 32);
  const int row8 = wave * 8 + (lane >> 3), cp = (lane & 7) ^ ((row8 >> 1) & 7);
  const unsigned avoff = (unsigned)(row8 * lda + cp * 8) * 2u, bvoff = (unsigned)(row8 * ldb + cp * 8) * 2u;
  const char* ab = (const char*)A;
  const char* bb = (const char*)B;
  const unsigned ldsw = (unsigned)(uintptr_t)lds + wave * 1024;
  auto issue = [&](int slot) {
#pragma unroll
    for (int i = 0; i < BM / 8 / NW; ++i) glds16s(avoff, ab + (size_t)i * NW * 8 * lda * 2, ldsw + slot * STG + i * NW * 1024);
#pragma unroll
    for (int i = 0; i < BN / 8 / NW; ++i) glds16s(bvoff, bb + (size_t)i * NW * 8 * ldb * 2, ldsw + slot * STG + BM * 128 + i * NW * 1024);
    ab += 128; bb += 128;
  };
  const int arow = wm * 32 + r, brow = wn * 32 + r;
  const int aoff = arow * 128 + ((hh ^ ((arow >> 1) & 1)) << 4), aswz = ((arow >> 1) & 6) << 4;
  const int boff = BM * 128 + brow * 128 + ((hh ^ ((brow >> 1) & 1)) << 4), bswz = ((brow >> 1) & 6) << 4;
  f32x16 acc = (f32x16){};
#pragma unroll
  for (int i = 0; i < NST - 1; ++i) issue(i);
  int slot = 0;
  for (int kt = 0; kt < nk; ++kt) {
    const int rem = nk - 1 - kt;
    if (rem >= NST - 2) asm volatile("s_waitcnt vmcnt(%0) lgkmcnt(0)\n\ts_barrier" ::"i"(NI * (NST - 2)) : "memory");
    else if (NST > 3 && rem == 2) asm volatile("s_waitcnt vmcnt(%0) lgkmcnt(0)\n\ts_barrier" ::"i"(NI * 2) : "memory");
    else if (rem == 1) asm volatile("s_waitcnt vmcnt(%0) lgkmcnt(0)\n\ts_barrier" ::"i"(NI) : "memory");
    else asm volatile("s_waitcnt vmcnt(0) lgkmcnt(0)\n\ts_barrier" ::: "memory");
    if (kt + NST - 1 < nk) issue(slot == 0 ? NST - 1 : slot - 1);
    const lds_char* sb = lds + slot * STG;
    slot = slot == NST - 1 ? 0 : slot + 1;
#pragma unroll
    for (int ks = 0; ks < 4; ++ks) {
      const bf16x8 af = *(const __attribute__((address_space(3))) bf16x8*)(sb + aoff + ((ks * 32) ^ aswz));
      const bf16x8 bf = *(const __attribute__((address_space(3))) bf16x8*)(sb + boff + ((ks * 32) ^ bswz));
      acc = MFMA32(bf, af, acc);
    }
  }
  return acc;
}
struct NtAdd { u32x2 v[4]; };
DI NtAdd nt_load(const u16* __restrict__ add, int m, int n0, int hh) {
  NtAdd r;
#pragma unroll
  for (int g = 0; g < 4; ++g) r.v[g] = *(const u32x2*)(add + (size_t)m * D_ + n0 + 8 * g + 4 * hh);
  return r;
}
DI void nt_store(const f32x16& acc, const NtAdd& ad, u16* __restrict__ out, int m, int n0, int hh) {
#pragma unroll
  for (int g = 0; g < 4; ++g) {
    const u32x2 xv = ad.v[g];
    *(u32x2*)(out + (size_t)m * D_ + n0 + 8 * g + 4 * hh) =
        (u32x2){cvtpk(acc[4 * g] + bflo(xv[0]), acc[4 * g + 1] + bfhi(xv[0])), cvtpk(acc[4 * g + 2] + bflo(xv[1]), acc[4 * g + 3] + bfhi(xv[1]))};
  }
}

constexpr int K2_LDS = 3 * (128 + 64) * 128;
__global__ __launch_bounds__(512, 4) void k2_kernel(const u16* __restrict__ Qb, const char* __restrict__ KV, u16* __restrict__ O,
                                                     const u16* __restrict__ H, const u16* __restrict__ wdT, const u16* __restrict__ x, u16* __restrict__ dp) {
  extern __shared__ __attribute__((aligned(16))) char smem[];
  const int bid = blockIdx.x;
  if (bid < 256) { attn_block(bid, Qb, KV, O, smem); return; }
  const int b2 = bid - 256, xcd = b2 & 7, j = b2 >> 3;
  const int m0 = (xcd * 2 + (j & 1)) * 128, n0 = (j >> 1) * 64;
  const int lane = threadIdx.x & 63, wave = __builtin_amdgcn_readfirstlane(threadIdx.x >> 6);
  const int m = m0 + (wave & 3) * 32 + (lane & 31), n = n0 + (wave >> 2) * 32;
  NtAdd ad = nt_load(x, m, n, lane >> 5);
  asm volatile("" : "+v"(ad.v[0]), "+v"(ad.v[1]), "+v"(ad.v[2]), "+v"(ad.v[3]));
  const f32x16 acc = nt_tile<128, 64, 3>(H + (size_t)m0 * F_, F_, wdT + (size_t)n0 * F_, F_, F_ / 64, (lds_char*)smem, wave, lane);
  nt_store(acc, ad, dp, m, n, lane >> 5);
}

constexpr int K3_LDS = 5 * (128 + 64) * 128;
__global__ __launch_bounds__(512, 2) void k3_kernel(const u16* __restrict__ Ob, const u16* __restrict__ woT, const u16* __restrict__ dp, u16* __restrict__ out) {
  extern __shared__ __attribute__((aligned(16))) char smem[];
  const int bid = blockIdx.x, xcd = bid & 7, j = bid >> 3;
  const int m0 = (xcd * 2 + (j & 1)) * 128, n0 = (j >> 1) * 64;
  const int lane = threadIdx.x & 63, wave = __builtin_amdgcn_readfirstlane(threadIdx.x >> 6);
  const int m = m0 + (wave & 3) * 32 + (lane & 31), n = n0 + (wave >> 2) * 32;
  const NtAdd ad = nt_load(dp, m, n, lane >> 5);
  const f32x16 acc = nt_tile<128, 64, 5>(Ob + (size_t)m0 * D_, D_, woT + (size_t)n0 * D_, D_, D_ / 64, (lds_char*)smem, wave, lane);
  nt_store(acc, ad, out, m, n, lane >> 5);
}

extern "C" void kernel_launch(void* const* d_in, const int* in_sizes, int n_in, void* d_out, int out_size, void* d_ws, size_t ws_size,
                              hipStream_t stream) {
  const u16* x = (const u16*)d_in[0];
  const u16* wqkv = (const u16*)d_in[1];
  const u16* wo = (const u16*)d_in[2];
  const u16* wg = (const u16*)d_in[4];
  const u16* wu = (const u16*)d_in[5];
  const u16* wd = (const u16*)d_in[6];
  char* ws = (char*)d_ws;
  u16* Qb = (u16*)(ws + WS_Q);
  char* KV = ws + WS_KV;
  u16* H = (u16*)(ws + WS_H);
  u16* Ob = (u16*)(ws + WS_O);
  u16* out = (u16*)d_out;
  u16* wdT = (u16*)(ws + WS_WDT);
  u16* woT = (u16*)(ws + WS_WOT);
  u16* dp = (u16*)(ws + WS_DP);
  (void)hipFuncSetAttribute((const void*)k1_kernel, hipFuncAttributeMaxDynamicSharedMemorySize, K1_LDS);
  (void)hipFuncSetAttribute((const void*)k2_kernel, hipFuncAttributeMaxDynamicSharedMemorySize, K2_LDS);
  (void)hipFuncSetAttribute((const void*)k3_kernel, hipFuncAttributeMaxDynamicSharedMemorySize, K3_LDS);
  k1_kernel<<<256, 512, K1_LDS, stream>>>(x, wqkv, wg, wu, Qb, KV, H, wd, wo, wdT, woT);
  k2_kernel<<<256 + 160, 512, K2_LDS, stream>>>(Qb, KV, Ob, H, wdT, x, dp);
  k3_kernel<<<160, 512, K3_LDS, stream>>>(Ob, woT, dp, out);
}
```
